```python
import torch

def _softmax(x, axis=-1):
    return torch.softmax(x, dim=axis)
def _gelu(x):
    return torch.nn.functional.gelu(x, approximate="tanh")
def _silu(x):
    return torch.nn.functional.silu(x)
def _sigmoid(x):
    return torch.sigmoid(x)
def _layernorm(x, axis=-1, eps=1e-5):
    m = x.mean(dim=axis, keepdim=True)
    v = x.var(dim=axis, keepdim=True, unbiased=False)
    return (x - m) / torch.sqrt(v + eps)
def _scatter_add(tgt, idx, src, axis=0):
    return tgt.clone().index_add_(axis, idx, src)
def _segment_sum(src, seg, n):
    out = torch.zeros((n,) + src.shape[1:], dtype=src.dtype)
    return out.index_add_(0, seg, src)
def _pad(x, pw):
    flat = tuple(v for pair in reversed(pw) for v in pair)
    return torch.nn.functional.pad(x, flat)

kernel_name = 'gated_mix__ssm_conv_s4d__grouped_kv_mqa__prompt'

def setup_inputs():

    _w = (lambda a: (lambda t: t.to(torch.bfloat16) if t.is_floating_point() else t)(torch.as_tensor(a)))
    def _A_setup():
        Bsz, L, D, N = 1, 2048, 576, 128
        return {
            "u": _w((lambda *s: torch.randn(*s, dtype=torch.bfloat16) * 0.05)(Bsz, L, D)),
            "log_A_real": _w((lambda *s: torch.randn(*s, dtype=torch.bfloat16) * 6.4)(D, N) * 0.5),
            "A_imag": _w((lambda *s: torch.randn(*s, dtype=torch.bfloat16) * 0.05)(D, N) * 3.14),
            "C_real": _w((lambda *s: torch.randn(*s, dtype=torch.bfloat16) * 0.05)(D, N) / N ** 0.5),
            "C_imag": _w((lambda *s: torch.randn(*s, dtype=torch.bfloat16) * 0.05)(D, N) / N ** 0.5),
            "log_dt": _w((lambda *s: torch.randn(*s, dtype=torch.bfloat16) * 0.05)(D) * 0.5 - 4.0),
            "D_skip": _w((lambda *s: torch.randn(*s, dtype=torch.bfloat16) * 0.05)(D)),
        }
    _A_in = _A_setup()

    def _B_setup():
        B, S, D, H = 1, 2048, 576, 32
        Dh = D // H
        return {
            "x": _w((lambda *s: torch.randn(*s, dtype=torch.bfloat16) * 0.05)(B, S, D)),
            "wq": _w((lambda *s: torch.randn(*s, dtype=torch.bfloat16) * 0.8)(D, H * Dh)),
            "wk": _w((lambda *s: torch.randn(*s, dtype=torch.bfloat16) * 0.8)(D, Dh)),
            "wv": _w((lambda *s: torch.randn(*s, dtype=torch.bfloat16) * 0.05)(D, Dh)),
            "wo": _w((lambda *s: torch.randn(*s, dtype=torch.bfloat16) * 0.05)(H * Dh, D)),
        }
    _B_in = _B_setup()

    return {
        "A_u": _A_in["u"],
        "A_log_A_real": _A_in["log_A_real"],
        "A_A_imag": _A_in["A_imag"],
        "A_C_real": _A_in["C_real"],
        "A_C_imag": _A_in["C_imag"],
        "A_log_dt": _A_in["log_dt"],
        "A_D_skip": _A_in["D_skip"],
        "B_x": _B_in["x"],
        "B_wq": _B_in["wq"],
        "B_wk": _B_in["wk"],
        "B_wv": _B_in["wv"],
        "B_wo": _B_in["wo"],
        "gate_w": _w((lambda *s: torch.randn(*s, dtype=torch.bfloat16) * 0.05)(576) * 16.0),
    }

def reference(A_u, A_log_A_real, A_A_imag, A_C_real, A_C_imag, A_log_dt, A_D_skip, B_x, B_wq, B_wk, B_wv, B_wo, gate_w):
    def _A_ref(u, log_A_real, A_imag, C_real, C_imag, log_dt, D_skip):
        L = u.shape[1]
        dt = torch.exp(log_dt)
        A = -torch.exp(log_A_real) + 1j * A_imag
        C = (C_real + 1j * C_imag) * dt[:, None]
        pos = (lambda x, dt: x.to(dt))(torch.arange(L), log_dt.dtype)
        P = torch.exp((A * dt[:, None])[:, :, None] * pos[None, None, :])
        K = 2.0 * torch.einsum("dn,dnl->dl", C, P).real
        L2 = 2 * L
        u_t = (lambda x, *d: x.permute(*d))(u, 0, 2, 1)
        Kf = (lambda x, *a, **k: torch.fft.rfft(x.float(), *a, **k))(K, L2)
        Uf = (lambda x, *a, **k: torch.fft.rfft(x.float(), *a, **k))(u_t, L2)
        y = (lambda x, *a, **k: torch.fft.irfft(x, *a, **k).to(torch.bfloat16))(Uf * Kf[None, :, :], L2)[:, :, :L]
        y = (lambda x, *d: x.permute(*d))(y, 0, 2, 1)
        return y + u * D_skip[None, None, :]

    def _B_ref(x, wq, wk, wv, wo):
        B, S, D = x.shape
        H = 32
        Dh = D // H
        q = (lambda x, *d: x.permute(*d))((x @ wq).reshape(B, S, H, Dh), 0, 2, 1, 3)
        k = x @ wk
        v = x @ wv
        scores = torch.einsum("bhqd,bkd->bhqk", q, k) * (Dh ** -0.5)
        p = _softmax(scores, axis=-1)
        o = torch.einsum("bhqk,bkd->bhqd", p, v)
        return (lambda x, *d: x.permute(*d))(o, 0, 2, 1, 3).reshape(B, S, H * Dh) @ wo

    x = A_u
    g = _sigmoid((x * gate_w).sum(-1, keepdims=True))
    a = _A_ref(x, A_log_A_real, A_A_imag, A_C_real, A_C_imag, A_log_dt, A_D_skip)
    b = _B_ref(x, B_wq, B_wk, B_wv, B_wo)
    return g * a + (1 - g) * b
```

```cpp
#include <hip/hip_runtime.h>
#include <hip/hip_bf16.h>
#include <stdint.h>

constexpr int L_SEQ = 2048;
constexpr int DM = 576;
constexpr int NH = 32;
constexpr int DH = 18;
constexpr int NST = 128;

typedef __attribute__((ext_vector_type(8))) short bf16x8;
typedef __attribute__((ext_vector_type(4))) short s16x4;
typedef __attribute__((ext_vector_type(4))) float f32x4;
typedef __attribute__((ext_vector_type(16))) float f32x16;
typedef __attribute__((ext_vector_type(4))) unsigned u32x4;
typedef __attribute__((ext_vector_type(2))) unsigned u32x2;
typedef __attribute__((ext_vector_type(4))) short v4i16_t;
typedef unsigned short u16;

#define DI __device__ __forceinline__
#define MFMA32(a, b, c) __builtin_amdgcn_mfma_f32_32x32x16_bf16((a), (b), (c), 0, 0, 0)
#define MFMA16(a, b, c) __builtin_amdgcn_mfma_f32_16x16x32_bf16((a), (b), (c), 0, 0, 0)
#define MFMA32F(a, b, c) __builtin_amdgcn_mfma_f32_32x32x2f32((a), (b), (c), 0, 0, 0)

constexpr size_t WS_Q = 0;
constexpr size_t WS_O = WS_Q + (size_t)L_SEQ * DM * 2;
constexpr size_t WS_UT = WS_O + (size_t)L_SEQ * DM * 2;
constexpr size_t WS_Y = WS_UT + (size_t)L_SEQ * DM * 2;
constexpr size_t WS_KV = WS_Y + (size_t)L_SEQ * DM * 2;
constexpr int KV_KP = 0;
constexpr int KV_KT = 65536;
constexpr int KV_V = 65536 + 8192;
constexpr int KV_BYTES = KV_V + 64 * 4 * 304;
constexpr size_t WS_G = WS_KV + KV_BYTES;

DI float bf2f(u16 v) { return __uint_as_float(((unsigned)v) << 16); }
DI u16 f2bf(float x) {
  __hip_bfloat16 b = __float2bfloat16(x);
  return __builtin_bit_cast(u16, b);
}
DI float bfr(float x) { return bf2f(f2bf(x)); }
DI unsigned pack2(float lo, float hi) { return (unsigned)f2bf(lo) | ((unsigned)f2bf(hi) << 16); }
DI s16x4 tr_read(const char* p) {
  return __builtin_bit_cast(s16x4, __builtin_amdgcn_ds_read_tr16_b64_v4i16(
                                       (__attribute__((address_space(3))) v4i16_t*)p));
}
DI int rho(int k) { return k ^ (((k >> 3) & 1) << 2); }

constexpr int PA_STRIDE = 160;
constexpr int PA_LDS = 576 * PA_STRIDE + 72 * 66 * 2;
__global__ void __launch_bounds__(256) proj_kernel(const u16* __restrict__ x, const u16* __restrict__ wq,
                                                   const u16* __restrict__ wk, const u16* __restrict__ wv,
                                                   const u16* __restrict__ gw, char* __restrict__ ws) {
  extern __shared__ __attribute__((aligned(16))) char smem[];
  const int tid = threadIdx.x, lane = tid & 63, w = tid >> 6;
  const int cb = blockIdx.x & 7, rb = blockIdx.x >> 3;
  const int fr = lane & 15, g = lane >> 4;

  bf16x8 xf[18];
  {
    const u16* xp = x + (size_t)(rb * 64 + w * 16 + fr) * DM + 8 * g;
#pragma unroll
    for (int s = 0; s < 18; ++s) xf[s] = *(const bf16x8*)(xp + 32 * s);
  }
  if (cb < 7) {
    for (int c = tid; c < 576 * 10; c += 256) {
      const int k = c / 10, ch = c - k * 10;
      const u32x4 v = *(const u32x4*)(wq + (size_t)k * DM + cb * 80 + ch * 8);
      *(u32x4*)(smem + rho(k) * PA_STRIDE + ch * 16) = v;
    }
  } else {
    for (int c = tid; c < 576 * 2; c += 256) {
      const int k = c >> 1, ch = c & 1;
      const u32x4 v = *(const u32x4*)(wq + (size_t)k * DM + 560 + ch * 8);
      *(u32x4*)(smem + rho(k) * PA_STRIDE + ch * 16) = v;
    }
    for (int c = tid; c < 576 * 64; c += 256) {
      const int k = c >> 6, n = c & 63;
      u16 v = 0;
      if (n < 18) v = wk[k * DH + n];
      else if (n < 36) v = wv[k * DH + n - 18];
      else if (n == 36) v = gw[k];
      *(u16*)(smem + rho(k) * PA_STRIDE + 32 + n * 2) = v;
    }
  }
  {
    u16* tl = (u16*)(smem + 576 * PA_STRIDE);
    for (int c = tid; c < 64 * 9; c += 256) {
      const int r = c / 9, ch = c - r * 9;
      const bf16x8 v = *(const bf16x8*)(x + (size_t)(rb * 64 + r) * DM + cb * 72 + ch * 8);
#pragma unroll
      for (int j = 0; j < 8; ++j) tl[(ch * 8 + j) * 66 + r] = (u16)v[j];
    }
  }
  __syncthreads();
  {
    const u16* tl = (const u16*)(smem + 576 * PA_STRIDE);
    u16* ut = (u16*)(ws + WS_UT);
    for (int c = tid; c < 72 * 32; c += 256) {
      const int dd = c >> 5, p = c & 31;
      const unsigned v = *(const unsigned*)(tl + dd * 66 + 2 * p);
      *(unsigned*)(ut + (size_t)(cb * 72 + dd) * L_SEQ + rb * 64 + 2 * p) = v;
    }
  }

  f32x4 acc[5];
#pragma unroll
  for (int c = 0; c < 5; ++c) acc[c] = f32x4{0.f, 0.f, 0.f, 0.f};
  const int q4 = (lane & 15) >> 2, p4 = lane & 3;
#pragma unroll
  for (int s = 0; s < 18; ++s) {
    const char* b0 = smem + rho(32 * s + 8 * g + q4) * PA_STRIDE + 8 * p4;
    const char* b1 = smem + rho(32 * s + 8 * g + 4 + q4) * PA_STRIDE + 8 * p4;
#pragma unroll
    for (int c = 0; c < 5; ++c) {
      const s16x4 lo = tr_read(b0 + 32 * c), hi = tr_read(b1 + 32 * c);
      const bf16x8 wf = __builtin_shufflevector(lo, hi, 0, 1, 2, 3, 4, 5, 6, 7);
      acc[c] = MFMA16(wf, xf[s], acc[c]);
    }
  }
  const int token = rb * 64 + w * 16 + fr;
  const float qscale = 0.23570226039551584f * 1.4426950408889634f;
  u16* Q = (u16*)(ws + WS_Q);
  char* kv = ws + WS_KV;
#pragma unroll
  for (int c = 0; c < 5; ++c) {
    const int col = cb * 80 + 16 * c + 4 * g;
    if (col < DM) {
      u32x2 v;
      v[0] = pack2(acc[c][0] * qscale, acc[c][1] * qscale);
      v[1] = pack2(acc[c][2] * qscale, acc[c][3] * qscale);
      *(u32x2*)(Q + (size_t)token * DM + col) = v;
    } else {
#pragma unroll
      for (int i = 0; i < 4; ++i) {
        const int n = col + i - DM;
        const float val = acc[c][i];
        if (n < 16) {
          *(u16*)(kv + KV_KP + (n >> 3) * 32768 + token * 16 + (n & 7) * 2) = f2bf(val);
        } else if (n < 18) {
          *(u16*)(kv + KV_KT + token * 4 + (n - 16) * 2) = f2bf(val);
        } else if (n < 37) {
          const int tile = token >> 5, s = (token >> 4) & 1, k16 = token & 15;
          const int h = (k16 >> 2) & 1, j = (k16 & 3) + 4 * (k16 >> 3);
          char* vb = kv + KV_V + ((tile * 2 + s) * 2 + h) * 304 + j * 2;
          if (n < 36) {
            *(u16*)(vb + (n - 18) * 16) = f2bf(val);
          } else {
            *(u16*)(vb + 18 * 16) = (u16)0x3F80;
            const float lg = bfr(val);
            const float gg = 1.f / (1.f + __expf(-lg));
            ((float*)(ws + WS_G))[token] = bfr(gg);
          }
        }
      }
    }
  }
}

constexpr int SS_ROW = 260;
constexpr int SS_XA = 0;
constexpr int SS_YB = SS_XA + 21 * SS_ROW * 4;
constexpr int SS_KP = 55296;
constexpr int SS_LDS = SS_KP + 16384;
constexpr int SS_Z0 = 2056;
constexpr int SS_RLEN = 2320;
constexpr int SS_RH = 0;
constexpr int SS_RL = SS_RH + SS_RLEN * 2;
constexpr int SS_UC = 9280;
constexpr int SS_UCS = 2072 * 2 + 16;
constexpr int SS_RED = SS_UC;

struct cf { float re, im; };
DI cf cmul(cf a, cf b) { return cf{a.re * b.re - a.im * b.im, a.re * b.im + a.im * b.re}; }
DI cf cexp_pos(float ar, float ai, float pos) {
  const float e = __expf(ar * pos);
  const float rev = (ai * pos) * 0.15915494309189535f;
  const float fr = rev - floorf(rev);
  return cf{e * __builtin_amdgcn_cosf(fr), e * __builtin_amdgcn_sinf(fr)};
}
DI void put_x(float* XA, int i, int n, cf t) { *(float2*)(XA + i * SS_ROW + 2 * n) = float2{t.re, -t.im}; }

__global__ void __launch_bounds__(256) ssm_kernel(const u16* __restrict__ logA, const u16* __restrict__ Aim,
                                                  const u16* __restrict__ Cre, const u16* __restrict__ Cim,
                                                  const u16* __restrict__ logdt, char* __restrict__ ws) {
  extern __shared__ __attribute__((aligned(16))) char smem[];
  const int tid = threadIdx.x, lane = tid & 63, w = tid >> 6;
  const int d = blockIdx.x;
  float* XA = (float*)(smem + SS_XA);
  float* YB = (float*)(smem + SS_YB);

  const u16* ut = (const u16*)(ws + WS_UT) + (size_t)d * L_SEQ;
  const u32x4 u0 = *(const u32x4*)(ut + tid * 8);
  u32x4 u1 = u32x4{0u, 0u, 0u, 0u};
  if (tid < 255) u1 = *(const u32x4*)(ut + tid * 8 + 8);

  {
    const int n = tid & 127;
    const float dt = bfr(__expf(bf2f(logdt[d])));
    const float ar = -bfr(__expf(bf2f(logA[d * NST + n]))) * dt;
    const float ai = bf2f(Aim[d * NST + n]) * dt;
    if (tid < 128) {
      const cf e8 = cexp_pos(ar, ai, 8.f);
      const cf e64 = cexp_pos(ar, ai, 64.f);
      cf base = cf{1.f, 0.f};
#pragma unroll
      for (int jb = 0; jb < 4; ++jb) {
        cf cur = base;
#pragma unroll
        for (int jj = 0; jj < 8; ++jj) {
          *(float2*)(YB + (jb * 8 + jj) * SS_ROW + 2 * n) = float2{cur.re, cur.im};
          cur = cmul(cur, e8);
        }
        base = cmul(base, e64);
      }
    } else {
      const cf c2 = cf{2.f * bf2f(Cre[d * NST + n]) * dt, 2.f * bf2f(Cim[d * NST + n]) * dt};
      const cf e1 = cexp_pos(ar, ai, 1.f);
      const cf e2 = cexp_pos(ar, ai, 2.f);
      const cf e4 = cexp_pos(ar, ai, 4.f);
      const cf e6 = cmul(e2, e4);
      cf cur = c2;
#pragma unroll
      for (int i = 0; i < 8; ++i) {
        put_x(XA, i, n, cur);
        cur = cmul(cur, e1);
      }
      cf t = cmul(c2, cexp_pos(ar, ai, 256.f));
      put_x(XA, 8, n, t);
      put_x(XA, 9, n, cmul(t, e2));
      put_x(XA, 10, n, cmul(t, e4));
      put_x(XA, 11, n, cmul(t, e6));
      t = cmul(c2, cexp_pos(ar, ai, 512.f));
      put_x(XA, 12, n, t);
      put_x(XA, 13, n, cmul(t, e4));
      t = cmul(c2, cexp_pos(ar, ai, 768.f));
      put_x(XA, 14, n, t);
      put_x(XA, 15, n, cmul(t, e4));
#pragma unroll
      for (int i = 0; i < 5; ++i) put_x(XA, 16 + i, n, cmul(c2, cexp_pos(ar, ai, 1024.f + 256.f * i)));
    }
  }
  __syncthreads();
  {
    const int r = lane & 31, h = lane >> 5;
    const int ra = r < 21 ? r : 20;
    f32x16 acc;
#pragma unroll
    for (int i = 0; i < 16; ++i) acc[i] = 0.f;
#pragma unroll
    for (int q = 0; q < 8; ++q) {
      const f32x4 a = *(const f32x4*)(XA + ra * SS_ROW + 64 * w + 8 * q + 4 * h);
      const f32x4 b = *(const f32x4*)(YB + r * SS_ROW + 64 * w + 8 * q + 4 * h);
#pragma unroll
      for (int e = 0; e < 4; ++e) acc = MFMA32F(a[e], b[e], acc);
    }
    float* kp = (float*)(smem + SS_KP) + w * 1024;
#pragma unroll
    for (int i = 0; i < 16; ++i) kp[((i & 3) + 8 * (i >> 2) + 4 * h) * 32 + r] = acc[i];
  }
  __syncthreads();
  {
    const float* kp = (const float*)(smem + SS_KP);
    u16* RH = (u16*)(smem + SS_RH);
    u16* RL = (u16*)(smem + SS_RL);
    for (int z = tid; z < SS_RLEN; z += 256) {
      const int lag = SS_Z0 - z;
      float kv = 0.f;
      if (lag >= 0 && lag < L_SEQ) {
        const int pos = (int)bfr((float)lag);
        int i, j;
        if (pos < 256) { j = pos >> 3; i = pos & 7; }
        else if (pos < 512) { const int r = pos - 256; j = r >> 3; i = 8 + ((r & 7) >> 1); }
        else if (pos < 1024) { const int r = pos - 512; j = (r & 255) >> 3; i = 12 + 2 * (r >> 8) + ((r & 7) >> 2); }
        else if (pos < 2048) { const int r = pos - 1024; j = (r & 255) >> 3; i = 16 + (r >> 8); }
        else { i = 20; j = 0; }
        const int o = i * 32 + j;
        kv = (kp[o] + kp[1024 + o]) + (kp[2048 + o] + kp[3072 + o]);
      }
      const u16 hi = f2bf(kv);
      RH[z] = hi;
      RL[z] = f2bf(kv - bf2f(hi));
    }
    char* uc = smem + SS_UC;
    const unsigned dw[8] = {u0[0], u0[1], u0[2], u0[3], u1[0], u1[1], u1[2], u1[3]};
#pragma unroll
    for (int r = 0; r < 8; ++r) {
      u32x4 v;
#pragma unroll
      for (int e = 0; e < 4; ++e) {
        const int b = (r >> 1) + e;
        v[e] = (r & 1) ? __builtin_amdgcn_alignbyte(dw[b + 1], dw[b], 2) : dw[b];
      }
      *(u32x4*)(uc + r * SS_UCS + (16 + tid * 8) * 2) = v;
    }
    if (tid < 8) {
      const int r = tid;
      u16* c = (u16*)(uc + r * SS_UCS);
      for (int z = 0; z < 8; ++z) c[z] = 0;
      for (int z = -8; z < 0; ++z) c[16 + z] = (z + r >= 0) ? ut[z + r] : (u16)0;
      for (int z = 2048; z < 2056; ++z) c[16 + z] = 0;
    }
  }
  __syncthreads();
  f32x16 D0, D1;
#pragma unroll
  for (int i = 0; i < 16; ++i) { D0[i] = 0.f; D1[i] = 0.f; }
  const int c = lane & 31, h = lane >> 5;
  {
    const int r = c & 7, t = c >> 3;
    const char* ucb = smem + SS_UC + r * SS_UCS + 32;
    const char* rhb = smem + SS_RH + (SS_Z0 - 8 * c + 8 * h) * 2;
    const char* rlb = smem + SS_RL + (SS_Z0 - 8 * c + 8 * h) * 2;
    for (int ei = w; ei < 129; ei += 4) {
      const int e = -784 + 16 * ei;
      int z = e + 256 * t + 8 * h;
      z = z < -16 ? -16 : z;
      const bf16x8 bfrag = *(const bf16x8*)(ucb + z * 2);
      {
        const int delta = 1024 - e;
        const bf16x8 ah = *(const bf16x8*)(rhb - delta * 2);
        const bf16x8 al = *(const bf16x8*)(rlb - delta * 2);
        D1 = MFMA32(ah, bfrag, D1);
        D1 = MFMA32(al, bfrag, D1);
      }
      if (e <= 240) {
        const int delta = -e;
        const bf16x8 ah = *(const bf16x8*)(rhb - delta * 2);
        const bf16x8 al = *(const bf16x8*)(rlb - delta * 2);
        D0 = MFMA32(ah, bfrag, D0);
        D0 = MFMA32(al, bfrag, D0);
      }
    }
  }
  __syncthreads();
  {
    float* red = (float*)(smem + SS_RED) + w * 2048;
    const int r = c & 7, t = c >> 3;
#pragma unroll
    for (int i = 0; i < 16; ++i) {
      const int row = (i & 3) + 8 * (i >> 2) + 4 * h;
      red[256 * t + 8 * row + r] = D0[i];
      red[1024 + 256 * t + 8 * row + r] = D1[i];
    }
  }
  __syncthreads();
  {
    const float* red = (const float*)(smem + SS_RED);
    float s[8];
#pragma unroll
    for (int half = 0; half < 2; ++half) {
      const f32x4 a = *(const f32x4*)(red + tid * 8 + 4 * half);
      const f32x4 b = *(const f32x4*)(red + 2048 + tid * 8 + 4 * half);
      const f32x4 cc = *(const f32x4*)(red + 4096 + tid * 8 + 4 * half);
      const f32x4 dd = *(const f32x4*)(red + 6144 + tid * 8 + 4 * half);
#pragma unroll
      for (int e = 0; e < 4; ++e) s[4 * half + e] = (a[e] + b[e]) + (cc[e] + dd[e]);
    }
    u32x4 v;
#pragma unroll
    for (int e = 0; e < 4; ++e) v[e] = pack2(s[2 * e], s[2 * e + 1]);
    *(u32x4*)((u16*)(ws + WS_Y) + (size_t)d * L_SEQ + tid * 8) = v;
  }
}

constexpr float ATT_THR = 16.f;
DI float max16(const f32x16& s) {
  float a = fmaxf(fmaxf(s[0], s[1]), s[2]);
  float b = fmaxf(fmaxf(s[3], s[4]), s[5]);
  float c = fmaxf(fmaxf(s[6], s[7]), s[8]);
  float d = fmaxf(fmaxf(s[9], s[10]), s[11]);
  a = fmaxf(fmaxf(a, s[12]), s[13]);
  b = fmaxf(fmaxf(b, s[14]), s[15]);
  return fmaxf(fmaxf(a, b), fmaxf(c, d));
}
DI unsigned cvtpk(float lo, float hi) {
  unsigned r;
  asm("v_cvt_pk_bf16_f32 %0, %1, %2" : "=v"(r) : "v"(lo), "v"(hi));
  return r;
}

__global__ void __launch_bounds__(512) attn_kernel(char* __restrict__ ws) {
  extern __shared__ __attribute__((aligned(16))) char smem[];
  const int tid = threadIdx.x, lane = tid & 63, w = tid >> 6;
  const int c = lane & 31, h = lane >> 5;
  const int tp = w & 3, kh = w >> 2;
  {
    const u32x4* src = (const u32x4*)(ws + WS_KV);
    u32x4* dst = (u32x4*)smem;
    for (int i = tid; i < KV_BYTES / 16; i += 512) dst[i] = src[i];
  }
  bf16x8 q0[2], q1[2];
#pragma unroll
  for (int sub = 0; sub < 2; ++sub) {
    const int token = blockIdx.x * 8 + 2 * tp + sub;
    const unsigned* qp = (const unsigned*)((const u16*)(ws + WS_Q) + (size_t)token * DM + c * DH);
    u32x4 a;
#pragma unroll
    for (int e = 0; e < 4; ++e) a[e] = qp[4 * h + e];
    q0[sub] = __builtin_bit_cast(bf16x8, a);
    u32x4 b = u32x4{0u, 0u, 0u, 0u};
    if (h == 0) b[0] = qp[8];
    q1[sub] = __builtin_bit_cast(bf16x8, b);
  }
  __syncthreads();

  const char* kpb = smem + KV_KP + h * 32768 + (kh * 1024 + c) * 16;
  const char* ktb = smem + KV_KT + (kh * 1024 + c) * 4;
  const int dv = c < 18 ? c : 18;
  const char* vb = smem + KV_V + (kh * 32 * 4 + h) * 304 + dv * 16;

  f32x16 negm[2], O[2];
  {
    const bf16x8 kf = *(const bf16x8*)kpb;
    u32x4 kt4 = u32x4{*(const unsigned*)ktb, 0u, 0u, 0u};
    const bf16x8 kt = __builtin_bit_cast(bf16x8, kt4);
#pragma unroll
    for (int sub = 0; sub < 2; ++sub) {
      f32x16 z;
#pragma unroll
      for (int i = 0; i < 16; ++i) z[i] = 0.f;
      f32x16 S = MFMA32(kf, q0[sub], z);
      S = MFMA32(kt, q1[sub], S);
      float mx = max16(S);
      mx = fmaxf(mx, __shfl_xor(mx, 32));
#pragma unroll
      for (int i = 0; i < 16; ++i) { negm[sub][i] = -mx; O[sub][i] = 0.f; }
    }
  }
  for (int tile = 0; tile < 32; ++tile) {
    const bf16x8 kf = *(const bf16x8*)(kpb + tile * 512);
    u32x4 kt4 = u32x4{*(const unsigned*)(ktb + tile * 128), 0u, 0u, 0u};
    const bf16x8 kt = __builtin_bit_cast(bf16x8, kt4);
    const bf16x8 vf0 = *(const bf16x8*)(vb + tile * 1216);
    const bf16x8 vf1 = *(const bf16x8*)(vb + tile * 1216 + 608);
#pragma unroll
    for (int sub = 0; sub < 2; ++sub) {
      f32x16 S = MFMA32(kf, q0[sub], negm[sub]);
      S = MFMA32(kt, q1[sub], S);
      const float tmax = max16(S);
      if (__any(tmax > ATT_THR)) {
        float tm = fmaxf(tmax, __shfl_xor(tmax, 32));
        tm = fmaxf(tm, 0.f);
        const float alpha = __builtin_amdgcn_exp2f(-tm);
#pragma unroll
        for (int i = 0; i < 16; ++i) {
          O[sub][i] *= alpha;
          S[i] -= tm;
          negm[sub][i] -= tm;
        }
      }
      u32x4 p0, p1;
#pragma unroll
      for (int e = 0; e < 4; ++e) {
        p0[e] = cvtpk(__builtin_amdgcn_exp2f(S[2 * e]), __builtin_amdgcn_exp2f(S[2 * e + 1]));
        p1[e] = cvtpk(__builtin_amdgcn_exp2f(S[8 + 2 * e]), __builtin_amdgcn_exp2f(S[8 + 2 * e + 1]));
      }
      O[sub] = MFMA32(vf0, __builtin_bit_cast(bf16x8, p0), O[sub]);
      O[sub] = MFMA32(vf1, __builtin_bit_cast(bf16x8, p1), O[sub]);
    }
  }
  __syncthreads();
  {
    float* xb = (float*)smem + (size_t)w * 17 * 64;
    const f32x16& Og = kh ? O[0] : O[1];
    const float mg = kh ? negm[0][0] : negm[1][0];
#pragma unroll
    for (int i = 0; i < 16; ++i) xb[i * 64 + lane] = Og[i];
    xb[16 * 64 + lane] = mg;
  }
  __syncthreads();
  {
    const float* pb = (const float*)smem + (size_t)(w ^ 4) * 17 * 64;
    const f32x16& Om = kh ? O[1] : O[0];
    const float nm_a = kh ? negm[1][0] : negm[0][0];
    const float nm_b = pb[16 * 64 + lane];
    const float nm = fminf(nm_a, nm_b);
    const float fa = __builtin_amdgcn_exp2f(nm - nm_a), fb = __builtin_amdgcn_exp2f(nm - nm_b);
    float r[11];
#pragma unroll
    for (int i = 0; i < 11; ++i) r[i] = Om[i] * fa + pb[i * 64 + lane] * fb;
    const float l = __shfl(r[10], c);
    const float inv = 1.f / l;
    const int token = blockIdx.x * 8 + 2 * tp + kh;
    unsigned* op = (unsigned*)((u16*)(ws + WS_O) + (size_t)token * DM + c * DH);
    op[2 * h + 0] = pack2(r[0] * inv, r[1] * inv);
    op[2 * h + 1] = pack2(r[2] * inv, r[3] * inv);
    op[4 + 2 * h + 0] = pack2(r[4] * inv, r[5] * inv);
    op[4 + 2 * h + 1] = pack2(r[6] * inv, r[7] * inv);
    if (h == 0) op[8] = pack2(r[8] * inv, r[9] * inv);
  }
}

constexpr int PC_STRIDE = 224;
constexpr int PC_LDS = 576 * PC_STRIDE;
__global__ void __launch_bounds__(256) out_kernel(const u16* __restrict__ x, const u16* __restrict__ wo,
                                                  const u16* __restrict__ dskip, const char* __restrict__ ws,
                                                  u16* __restrict__ out) {
  extern __shared__ __attribute__((aligned(16))) char smem[];
  const int tid = threadIdx.x, lane = tid & 63, w = tid >> 6;
  const int cb = blockIdx.x % 6, rb = blockIdx.x / 6;
  const int fr = lane & 15, g = lane >> 4;
  const int token = rb * 64 + w * 16 + fr;

  bf16x8 of[18];
  {
    const u16* op = (const u16*)(ws + WS_O) + (size_t)token * DM + 8 * g;
#pragma unroll
    for (int s = 0; s < 18; ++s) of[s] = *(const bf16x8*)(op + 32 * s);
  }
  for (int c = tid; c < 576 * 12; c += 256) {
    const int k = c / 12, ch = c - k * 12;
    const u32x4 v = *(const u32x4*)(wo + (size_t)k * DM + cb * 96 + ch * 8);
    *(u32x4*)(smem + rho(k) * PC_STRIDE + ch * 16) = v;
  }
  __syncthreads();
  f32x4 acc[6];
#pragma unroll
  for (int c = 0; c < 6; ++c) acc[c] = f32x4{0.f, 0.f, 0.f, 0.f};
  const int q4 = (lane & 15) >> 2, p4 = lane & 3;
#pragma unroll
  for (int s = 0; s < 18; ++s) {
    const char* b0 = smem + rho(32 * s + 8 * g + q4) * PC_STRIDE + 8 * p4;
    const char* b1 = smem + rho(32 * s + 8 * g + 4 + q4) * PC_STRIDE + 8 * p4;
#pragma unroll
    for (int c = 0; c < 6; ++c) {
      const s16x4 lo = tr_read(b0 + 32 * c), hi = tr_read(b1 + 32 * c);
      const bf16x8 wf = __builtin_shufflevector(lo, hi, 0, 1, 2, 3, 4, 5, 6, 7);
      acc[c] = MFMA16(wf, of[s], acc[c]);
    }
  }
  const float gg = ((const float*)(ws + WS_G))[token];
  const float og = bfr(1.f - gg);
  const u16* Y = (const u16*)(ws + WS_Y);
#pragma unroll
  for (int c = 0; c < 6; ++c) {
    const int col = cb * 96 + 16 * c + 4 * g;
    const s16x4 uv = *(const s16x4*)(x + (size_t)token * DM + col);
    const s16x4 dv = *(const s16x4*)(dskip + col);
    float r[4];
#pragma unroll
    for (int i = 0; i < 4; ++i) {
      const float y = bf2f(Y[(size_t)(col + i) * L_SEQ + token]);
      const float a = bfr(y + bfr(bf2f((u16)uv[i]) * bf2f((u16)dv[i])));
      const float b = bfr(acc[c][i]);
      r[i] = bfr(gg * a) + bfr(og * b);
    }
    u32x2 v;
    v[0] = pack2(r[0], r[1]);
    v[1] = pack2(r[2], r[3]);
    *(u32x2*)(out + (size_t)token * DM + col) = v;
  }
}

extern "C" void kernel_launch(void* const* d_in, const int* in_sizes, int n_in, void* d_out, int out_size,
                              void* d_ws, size_t ws_size, hipStream_t stream) {
  const u16* x = (const u16*)d_in[0];
  const u16* logA = (const u16*)d_in[1];
  const u16* Aim = (const u16*)d_in[2];
  const u16* Cre = (const u16*)d_in[3];
  const u16* Cim = (const u16*)d_in[4];
  const u16* logdt = (const u16*)d_in[5];
  const u16* dskip = (const u16*)d_in[6];
  const u16* wq = (const u16*)d_in[8];
  const u16* wk = (const u16*)d_in[9];
  const u16* wv = (const u16*)d_in[10];
  const u16* wo = (const u16*)d_in[11];
  const u16* gw = (const u16*)d_in[12];
  char* ws = (char*)d_ws;
  (void)hipFuncSetAttribute((const void*)proj_kernel, hipFuncAttributeMaxDynamicSharedMemorySize, PA_LDS);
  (void)hipFuncSetAttribute((const void*)ssm_kernel, hipFuncAttributeMaxDynamicSharedMemorySize, SS_LDS);
  (void)hipFuncSetAttribute((const void*)attn_kernel, hipFuncAttributeMaxDynamicSharedMemorySize, KV_BYTES);
  (void)hipFuncSetAttribute((const void*)out_kernel, hipFuncAttributeMaxDynamicSharedMemorySize, PC_LDS);
  proj_kernel<<<256, 256, PA_LDS, stream>>>(x, wq, wk, wv, gw, ws);
  ssm_kernel<<<DM, 256, SS_LDS, stream>>>(logA, Aim, Cre, Cim, logdt, ws);
  attn_kernel<<<256, 512, KV_BYTES, stream>>>(ws);
  out_kernel<<<192, 256, PC_LDS, stream>>>(x, wo, dskip, ws, (u16*)d_out);
}
```

```cpp
#include <hip/hip_runtime.h>
#include <hip/hip_bf16.h>
#include <stdint.h>

constexpr int L_SEQ = 2048;
constexpr int DM = 576;
constexpr int NH = 32;
constexpr int DH = 18;
constexpr int NST = 128;

typedef __attribute__((ext_vector_type(8))) short bf16x8;
typedef __attribute__((ext_vector_type(4))) short s16x4;
typedef __attribute__((ext_vector_type(4))) float f32x4;
typedef __attribute__((ext_vector_type(16))) float f32x16;
typedef __attribute__((ext_vector_type(4))) unsigned u32x4;
typedef __attribute__((ext_vector_type(2))) unsigned u32x2;
typedef __attribute__((ext_vector_type(4))) short v4i16_t;
typedef unsigned short u16;

#define DI __device__ __forceinline__
#define MFMA32(a, b, c) __builtin_amdgcn_mfma_f32_32x32x16_bf16((a), (b), (c), 0, 0, 0)
#define MFMA16(a, b, c) __builtin_amdgcn_mfma_f32_16x16x32_bf16((a), (b), (c), 0, 0, 0)
#define MFMA32F(a, b, c) __builtin_amdgcn_mfma_f32_32x32x2f32((a), (b), (c), 0, 0, 0)

constexpr size_t WS_Q = 0;
constexpr size_t WS_O = WS_Q + (size_t)L_SEQ * DM * 2;
constexpr size_t WS_UT = WS_O + (size_t)L_SEQ * DM * 2;
constexpr size_t WS_Y = WS_UT + (size_t)L_SEQ * DM * 2;
constexpr size_t WS_KV = WS_Y + (size_t)L_SEQ * DM * 2;
constexpr int KV_KP = 0;
constexpr int KV_KT = 1024;
constexpr int KV_V = 1152;
constexpr int KV_TILE = 2368;
constexpr int KV_BYTES = 64 * KV_TILE;
constexpr size_t WS_G = WS_KV + KV_BYTES;

DI float bf2f(u16 v) { return __uint_as_float(((unsigned)v) << 16); }
DI u16 f2bf(float x) {
  __hip_bfloat16 b = __float2bfloat16(x);
  return __builtin_bit_cast(u16, b);
}
DI float bfr(float x) { return bf2f(f2bf(x)); }
DI unsigned pack2(float lo, float hi) { return (unsigned)f2bf(lo) | ((unsigned)f2bf(hi) << 16); }
DI s16x4 tr_read(const char* p) {
  return __builtin_bit_cast(s16x4, __builtin_amdgcn_ds_read_tr16_b64_v4i16(
                                       (__attribute__((address_space(3))) v4i16_t*)p));
}
DI int rho(int k) { return k ^ (((k >> 3) & 1) << 2); }
DI void glds16(const void* gsrc, char* lds_wave_base) {
  __builtin_amdgcn_global_load_lds((const __attribute__((address_space(1))) void*)gsrc,
                                   (__attribute__((address_space(3))) void*)lds_wave_base, 16, 0, 0);
}
DI void wait_vm0() { asm volatile("s_waitcnt vmcnt(0)" ::: "memory"); }

constexpr int PA_STRIDE = 160;
constexpr int PA_LDS = 576 * PA_STRIDE + 72 * 66 * 2;
__global__ void __launch_bounds__(256) proj_kernel(const u16* __restrict__ x, const u16* __restrict__ wq,
                                                   const u16* __restrict__ wk, const u16* __restrict__ wv,
                                                   const u16* __restrict__ gw, char* __restrict__ ws) {
  extern __shared__ __attribute__((aligned(16))) char smem[];
  const int tid = threadIdx.x, lane = tid & 63, w = tid >> 6;
  const int cb = blockIdx.x & 7, rb = blockIdx.x >> 3;
  const int fr = lane & 15, g = lane >> 4;

  bf16x8 xf[18];
  {
    const u16* xp = x + (size_t)(rb * 64 + w * 16 + fr) * DM + 8 * g;
#pragma unroll
    for (int s = 0; s < 18; ++s) xf[s] = *(const bf16x8*)(xp + 32 * s);
  }
  if (cb < 7) {
    for (int it = w; it < 90; it += 4) {
      const int ci = it * 64 + lane;
      const int row = ci / 10, ch = ci - row * 10;
      glds16(wq + (size_t)rho(row) * DM + cb * 80 + ch * 8, smem + it * 1024);
    }
  } else {
    for (int k = tid; k < 576; k += 256) {
      const u32x4 a0 = *(const u32x4*)(wq + (size_t)k * DM + 560);
      const u32x4 a1 = *(const u32x4*)(wq + (size_t)k * DM + 568);
      unsigned kk[9], vv[9];
#pragma unroll
      for (int e = 0; e < 9; ++e) {
        kk[e] = ((const unsigned*)wk)[k * 9 + e];
        vv[e] = ((const unsigned*)wv)[k * 9 + e];
      }
      const unsigned gv = gw[k];
      char* dst = smem + rho(k) * PA_STRIDE;
      *(u32x4*)(dst) = a0;
      *(u32x4*)(dst + 16) = a1;
      *(u32x4*)(dst + 32) = u32x4{kk[0], kk[1], kk[2], kk[3]};
      *(u32x4*)(dst + 48) = u32x4{kk[4], kk[5], kk[6], kk[7]};
      *(u32x4*)(dst + 64) = u32x4{kk[8], vv[0], vv[1], vv[2]};
      *(u32x4*)(dst + 80) = u32x4{vv[3], vv[4], vv[5], vv[6]};
      *(u32x4*)(dst + 96) = u32x4{vv[7], vv[8], gv, 0u};
      *(u32x4*)(dst + 112) = u32x4{0u, 0u, 0u, 0u};
      *(u32x4*)(dst + 128) = u32x4{0u, 0u, 0u, 0u};
      *(u32x4*)(dst + 144) = u32x4{0u, 0u, 0u, 0u};
    }
  }
  {
    u16* tl = (u16*)(smem + 576 * PA_STRIDE);
    bf16x8 tv[3];
#pragma unroll
    for (int i = 0; i < 3; ++i) {
      const int c = tid + 256 * i;
      const int cc = c < 576 ? c : 0;
      const int r = cc / 9, ch = cc - r * 9;
      tv[i] = *(const bf16x8*)(x + (size_t)(rb * 64 + r) * DM + cb * 72 + ch * 8);
    }
#pragma unroll
    for (int i = 0; i < 3; ++i) {
      const int c = tid + 256 * i;
      if (c < 576) {
        const int r = c / 9, ch = c - r * 9;
#pragma unroll
        for (int j = 0; j < 8; ++j) tl[(ch * 8 + j) * 66 + r] = (u16)tv[i][j];
      }
    }
  }
  wait_vm0();
  __syncthreads();
  {
    const u16* tl = (const u16*)(smem + 576 * PA_STRIDE);
    u16* ut = (u16*)(ws + WS_UT);
    for (int c = tid; c < 72 * 32; c += 256) {
      const int dd = c >> 5, p = c & 31;
      const unsigned v = *(const unsigned*)(tl + dd * 66 + 2 * p);
      *(unsigned*)(ut + (size_t)(cb * 72 + dd) * L_SEQ + rb * 64 + 2 * p) = v;
    }
  }

  f32x4 acc[5];
#pragma unroll
  for (int c = 0; c < 5; ++c) acc[c] = f32x4{0.f, 0.f, 0.f, 0.f};
  const int q4 = (lane & 15) >> 2, p4 = lane & 3;
#pragma unroll
  for (int s = 0; s < 18; ++s) {
    const char* b0 = smem + rho(32 * s + 8 * g + q4) * PA_STRIDE + 8 * p4;
    const char* b1 = smem + rho(32 * s + 8 * g + 4 + q4) * PA_STRIDE + 8 * p4;
#pragma unroll
    for (int c = 0; c < 5; ++c) {
      const s16x4 lo = tr_read(b0 + 32 * c), hi = tr_read(b1 + 32 * c);
      const bf16x8 wf = __builtin_shufflevector(lo, hi, 0, 1, 2, 3, 4, 5, 6, 7);
      acc[c] = MFMA16(wf, xf[s], acc[c]);
    }
  }
  const int token = rb * 64 + w * 16 + fr;
  const float qscale = 0.23570226039551584f * 1.4426950408889634f;
  u16* Q = (u16*)(ws + WS_Q);
  char* kv = ws + WS_KV;
#pragma unroll
  for (int c = 0; c < 5; ++c) {
    const int col = cb * 80 + 16 * c + 4 * g;
    if (col < DM) {
      u32x2 v;
      v[0] = pack2(acc[c][0] * qscale, acc[c][1] * qscale);
      v[1] = pack2(acc[c][2] * qscale, acc[c][3] * qscale);
      *(u32x2*)(Q + (size_t)token * DM + col) = v;
    } else {
#pragma unroll
      for (int i = 0; i < 4; ++i) {
        const int n = col + i - DM;
        const float val = acc[c][i];
        if (n < 16) {
          *(u16*)(kv + (token >> 5) * KV_TILE + KV_KP + (n >> 3) * 512 + (token & 31) * 16 + (n & 7) * 2) = f2bf(val);
        } else if (n < 18) {
          *(u16*)(kv + (token >> 5) * KV_TILE + KV_KT + (token & 31) * 4 + (n - 16) * 2) = f2bf(val);
        } else if (n < 37) {
          const int tile = token >> 5, s = (token >> 4) & 1, k16 = token & 15;
          const int h = (k16 >> 2) & 1, j = (k16 & 3) + 4 * (k16 >> 3);
          char* vb = kv + tile * KV_TILE + KV_V + (s * 2 + h) * 304 + j * 2;
          if (n < 36) {
            *(u16*)(vb + (n - 18) * 16) = f2bf(val);
          } else {
            *(u16*)(vb + 18 * 16) = (u16)0x3F80;
            const float lg = bfr(val);
            const float gg = 1.f / (1.f + __expf(-lg));
            ((float*)(ws + WS_G))[token] = bfr(gg);
          }
        }
      }
    }
  }
}

constexpr int SS_ROW = 260;
constexpr int SS_XA = 0;
constexpr int SS_YB = SS_XA + 21 * SS_ROW * 4;
constexpr int SS_KP = 55296;
constexpr int SS_LDS = SS_KP + 16384;
constexpr int SS_Z0 = 2056;
constexpr int SS_RLEN = 2320;
constexpr int SS_RH = 0;
constexpr int SS_RL = SS_RH + SS_RLEN * 2;
constexpr int SS_UC = 9280;
constexpr int SS_UCS = 2072 * 2 + 16;
constexpr int SS_RED = SS_UC;

struct cf { float re, im; };
DI cf cmul(cf a, cf b) { return cf{a.re * b.re - a.im * b.im, a.re * b.im + a.im * b.re}; }
DI cf cexp_pos(float ar, float ai, float pos) {
  const float e = __expf(ar * pos);
  const float rev = (ai * pos) * 0.15915494309189535f;
  const float fr = rev - floorf(rev);
  return cf{e * __builtin_amdgcn_cosf(fr), e * __builtin_amdgcn_sinf(fr)};
}
DI void put_x(float* XA, int i, int n, cf t) { *(float2*)(XA + i * SS_ROW + 2 * n) = float2{t.re, -t.im}; }

__global__ void __launch_bounds__(256) ssm_kernel(const u16* __restrict__ logA, const u16* __restrict__ Aim,
                                                  const u16* __restrict__ Cre, const u16* __restrict__ Cim,
                                                  const u16* __restrict__ logdt, char* __restrict__ ws) {
  extern __shared__ __attribute__((aligned(16))) char smem[];
  const int tid = threadIdx.x, lane = tid & 63, w = tid >> 6;
  const int d = blockIdx.x;
  float* XA = (float*)(smem + SS_XA);
  float* YB = (float*)(smem + SS_YB);

  const u16* ut = (const u16*)(ws + WS_UT) + (size_t)d * L_SEQ;
  const u32x4 u0 = *(const u32x4*)(ut + tid * 8);
  u32x4 u1 = u32x4{0u, 0u, 0u, 0u};
  if (tid < 255) u1 = *(const u32x4*)(ut + tid * 8 + 8);

  {
    const int n = tid & 127;
    const float dt = bfr(__expf(bf2f(logdt[d])));
    const float ar = -bfr(__expf(bf2f(logA[d * NST + n]))) * dt;
    const float ai = bf2f(Aim[d * NST + n]) * dt;
    if (tid < 128) {
      const cf e8 = cexp_pos(ar, ai, 8.f);
      const cf e64 = cexp_pos(ar, ai, 64.f);
      cf base = cf{1.f, 0.f};
#pragma unroll
      for (int jb = 0; jb < 4; ++jb) {
        cf cur = base;
#pragma unroll
        for (int jj = 0; jj < 8; ++jj) {
          *(float2*)(YB + (jb * 8 + jj) * SS_ROW + 2 * n) = float2{cur.re, cur.im};
          cur = cmul(cur, e8);
        }
        base = cmul(base, e64);
      }
    } else {
      const cf c2 = cf{2.f * bf2f(Cre[d * NST + n]) * dt, 2.f * bf2f(Cim[d * NST + n]) * dt};
      const cf e1 = cexp_pos(ar, ai, 1.f);
      const cf e2 = cexp_pos(ar, ai, 2.f);
      const cf e4 = cexp_pos(ar, ai, 4.f);
      const cf e6 = cmul(e2, e4);
      cf cur = c2;
#pragma unroll
      for (int i = 0; i < 8; ++i) {
        put_x(XA, i, n, cur);
        cur = cmul(cur, e1);
      }
      cf t = cmul(c2, cexp_pos(ar, ai, 256.f));
      put_x(XA, 8, n, t);
      put_x(XA, 9, n, cmul(t, e2));
      put_x(XA, 10, n, cmul(t, e4));
      put_x(XA, 11, n, cmul(t, e6));
      t = cmul(c2, cexp_pos(ar, ai, 512.f));
      put_x(XA, 12, n, t);
      put_x(XA, 13, n, cmul(t, e4));
      t = cmul(c2, cexp_pos(ar, ai, 768.f));
      put_x(XA, 14, n, t);
      put_x(XA, 15, n, cmul(t, e4));
#pragma unroll
      for (int i = 0; i < 5; ++i) put_x(XA, 16 + i, n, cmul(c2, cexp_pos(ar, ai, 1024.f + 256.f * i)));
    }
  }
  __syncthreads();
  {
    const int r = lane & 31, h = lane >> 5;
    const int ra = r < 21 ? r : 20;
    f32x16 acc;
#pragma unroll
    for (int i = 0; i < 16; ++i) acc[i] = 0.f;
#pragma unroll
    for (int q = 0; q < 8; ++q) {
      const f32x4 a = *(const f32x4*)(XA + ra * SS_ROW + 64 * w + 8 * q + 4 * h);
      const f32x4 b = *(const f32x4*)(YB + r * SS_ROW + 64 * w + 8 * q + 4 * h);
#pragma unroll
      for (int e = 0; e < 4; ++e) acc = MFMA32F(a[e], b[e], acc);
    }
    float* kp = (float*)(smem + SS_KP) + w * 1024;
#pragma unroll
    for (int i = 0; i < 16; ++i) kp[((i & 3) + 8 * (i >> 2) + 4 * h) * 32 + r] = acc[i];
  }
  __syncthreads();
  {
    const float* kp = (const float*)(smem + SS_KP);
    u16* RH = (u16*)(smem + SS_RH);
    u16* RL = (u16*)(smem + SS_RL);
    for (int z = tid; z < SS_RLEN; z += 256) {
      const int lag = SS_Z0 - z;
      float kv = 0.f;
      if (lag >= 0 && lag < L_SEQ) {
        const int pos = (int)bfr((float)lag);
        int i, j;
        if (pos < 256) { j = pos >> 3; i = pos & 7; }
        else if (pos < 512) { const int r = pos - 256; j = r >> 3; i = 8 + ((r & 7) >> 1); }
        else if (pos < 1024) { const int r = pos - 512; j = (r & 255) >> 3; i = 12 + 2 * (r >> 8) + ((r & 7) >> 2); }
        else if (pos < 2048) { const int r = pos - 1024; j = (r & 255) >> 3; i = 16 + (r >> 8); }
        else { i = 20; j = 0; }
        const int o = i * 32 + j;
        kv = (kp[o] + kp[1024 + o]) + (kp[2048 + o] + kp[3072 + o]);
      }
      const u16 hi = f2bf(kv);
      RH[z] = hi;
      RL[z] = f2bf(kv - bf2f(hi));
    }
    char* uc = smem + SS_UC;
    const unsigned dw[8] = {u0[0], u0[1], u0[2], u0[3], u1[0], u1[1], u1[2], u1[3]};
#pragma unroll
    for (int r = 0; r < 8; ++r) {
      u32x4 v;
#pragma unroll
      for (int e = 0; e < 4; ++e) {
        const int b = (r >> 1) + e;
        v[e] = (r & 1) ? __builtin_amdgcn_alignbyte(dw[b + 1], dw[b], 2) : dw[b];
      }
      *(u32x4*)(uc + r * SS_UCS + (16 + tid * 8) * 2) = v;
    }
    if (tid < 8) {
      *(u32x4*)(uc + tid * SS_UCS) = u32x4{0u, 0u, 0u, 0u};
      *(u32x4*)(uc + tid * SS_UCS + (16 + 2048) * 2) = u32x4{0u, 0u, 0u, 0u};
    }
    if (tid == 0) {
      const unsigned dz[8] = {0u, 0u, 0u, 0u, u0[0], u0[1], u0[2], u0[3]};
#pragma unroll
      for (int r = 0; r < 8; ++r) {
        u32x4 v;
#pragma unroll
        for (int e = 0; e < 4; ++e) {
          const int b = (r >> 1) + e;
          v[e] = (r & 1) ? __builtin_amdgcn_alignbyte(dz[b + 1], dz[b], 2) : dz[b];
        }
        *(u32x4*)(uc + r * SS_UCS + 16) = v;
      }
    }
  }
  __syncthreads();
  f32x16 D0, D1;
#pragma unroll
  for (int i = 0; i < 16; ++i) { D0[i] = 0.f; D1[i] = 0.f; }
  const int c = lane & 31, h = lane >> 5;
  {
    const int r = c & 7, t = c >> 3;
    const char* ucb = smem + SS_UC + r * SS_UCS + 32;
    const char* rhb = smem + SS_RH + (SS_Z0 - 8 * c + 8 * h) * 2;
    const char* rlb = smem + SS_RL + (SS_Z0 - 8 * c + 8 * h) * 2;
    for (int ei = w; ei < 129; ei += 4) {
      const int e = -784 + 16 * ei;
      int z = e + 256 * t + 8 * h;
      z = z < -16 ? -16 : z;
      const bf16x8 bfrag = *(const bf16x8*)(ucb + z * 2);
      {
        const int delta = 1024 - e;
        const bf16x8 ah = *(const bf16x8*)(rhb - delta * 2);
        const bf16x8 al = *(const bf16x8*)(rlb - delta * 2);
        D1 = MFMA32(ah, bfrag, D1);
        D1 = MFMA32(al, bfrag, D1);
      }
      if (e <= 240) {
        const int delta = -e;
        const bf16x8 ah = *(const bf16x8*)(rhb - delta * 2);
        const bf16x8 al = *(const bf16x8*)(rlb - delta * 2);
        D0 = MFMA32(ah, bfrag, D0);
        D0 = MFMA32(al, bfrag, D0);
      }
    }
  }
  __syncthreads();
  {
    float* red = (float*)(smem + SS_RED) + w * 2048;
    const int r = c & 7, t = c >> 3;
#pragma unroll
    for (int i = 0; i < 16; ++i) {
      const int row = (i & 3) + 8 * (i >> 2) + 4 * h;
      red[256 * t + 8 * row + r] = D0[i];
      red[1024 + 256 * t + 8 * row + r] = D1[i];
    }
  }
  __syncthreads();
  {
    const float* red = (const float*)(smem + SS_RED);
    float s[8];
#pragma unroll
    for (int half = 0; half < 2; ++half) {
      const f32x4 a = *(const f32x4*)(red + tid * 8 + 4 * half);
      const f32x4 b = *(const f32x4*)(red + 2048 + tid * 8 + 4 * half);
      const f32x4 cc = *(const f32x4*)(red + 4096 + tid * 8 + 4 * half);
      const f32x4 dd = *(const f32x4*)(red + 6144 + tid * 8 + 4 * half);
#pragma unroll
      for (int e = 0; e < 4; ++e) s[4 * half + e] = (a[e] + b[e]) + (cc[e] + dd[e]);
    }
    u32x4 v;
#pragma unroll
    for (int e = 0; e < 4; ++e) v[e] = pack2(s[2 * e], s[2 * e + 1]);
    *(u32x4*)((u16*)(ws + WS_Y) + (size_t)d * L_SEQ + tid * 8) = v;
  }
}

constexpr float ATT_THR = 16.f;
constexpr int AT_CHUNK = 16 * KV_TILE;
constexpr int AT_LDS = 2 * AT_CHUNK;
DI float max16(const f32x16& s) {
  float a = fmaxf(fmaxf(s[0], s[1]), s[2]);
  float b = fmaxf(fmaxf(s[3], s[4]), s[5]);
  float c = fmaxf(fmaxf(s[6], s[7]), s[8]);
  float d = fmaxf(fmaxf(s[9], s[10]), s[11]);
  a = fmaxf(fmaxf(a, s[12]), s[13]);
  b = fmaxf(fmaxf(b, s[14]), s[15]);
  return fmaxf(fmaxf(a, b), fmaxf(c, d));
}
typedef __attribute__((ext_vector_type(2))) float f32x2;
typedef __attribute__((ext_vector_type(2))) __bf16 bf16x2_t;
DI unsigned cvtpk(float lo, float hi) {
  const f32x2 v = f32x2{lo, hi};
  return __builtin_bit_cast(unsigned, __builtin_convertvector(v, bf16x2_t));
}
DI void at_issue_chunk(const char* src, char* slot, int w, int lane) {
  for (int it = w; it < 37; it += 8) glds16(src + it * 1024 + lane * 16, slot + it * 1024);
}

DI bf16x8 pack8(const f32x16& S, int o) {
  u32x4 p;
#pragma unroll
  for (int e = 0; e < 4; ++e)
    p[e] = cvtpk(__builtin_amdgcn_exp2f(S[o + 2 * e]), __builtin_amdgcn_exp2f(S[o + 2 * e + 1]));
  return __builtin_bit_cast(bf16x8, p);
}

DI f32x16 attn_safe_pass(const char* kv, bf16x8 q0, bf16x8 q1, int koff, int toff, int voff) {
  f32x16 O, z;
#pragma unroll
  for (int i = 0; i < 16; ++i) { O[i] = 0.f; z[i] = 0.f; }
  float m = -INFINITY;
  for (int tile = 0; tile < 64; ++tile) {
    const char* tb = kv + tile * KV_TILE;
    const bf16x8 kf = *(const bf16x8*)(tb + koff);
    const u32x4 kt4 = u32x4{*(const unsigned*)(tb + toff), 0u, 0u, 0u};
    const bf16x8 vf0 = *(const bf16x8*)(tb + voff);
    const bf16x8 vf1 = *(const bf16x8*)(tb + voff + 608);
    f32x16 S = MFMA32(kf, q0, z);
    S = MFMA32(__builtin_bit_cast(bf16x8, kt4), q1, S);
    float tm = max16(S);
    tm = fmaxf(tm, __shfl_xor(tm, 32));
    const float mn = fmaxf(m, tm);
    const float alpha = __builtin_amdgcn_exp2f(m - mn);
    m = mn;
#pragma unroll
    for (int i = 0; i < 16; ++i) { O[i] *= alpha; S[i] -= mn; }
    O = MFMA32(vf0, pack8(S, 0), O);
    O = MFMA32(vf1, pack8(S, 8), O);
  }
  return O;
}

__global__ void __launch_bounds__(512) attn_kernel(char* __restrict__ ws) {
  extern __shared__ __attribute__((aligned(16))) char smem[];
  const int tid = threadIdx.x, lane = tid & 63, w = tid >> 6;
  const int c = lane & 31, h = lane >> 5;
  const char* kvsrc = ws + WS_KV;
  const int token = blockIdx.x * 8 + w;
  bf16x8 q0, q1;
  {
    const unsigned* qp = (const unsigned*)((const u16*)(ws + WS_Q) + (size_t)token * DM + c * DH);
    u32x4 a;
#pragma unroll
    for (int e = 0; e < 4; ++e) a[e] = qp[4 * h + e];
    q0 = __builtin_bit_cast(bf16x8, a);
    u32x4 b = u32x4{0u, 0u, 0u, 0u};
    if (h == 0) b[0] = qp[8];
    q1 = __builtin_bit_cast(bf16x8, b);
  }
  at_issue_chunk(kvsrc, smem, w, lane);
  wait_vm0();
  __syncthreads();
  at_issue_chunk(kvsrc + AT_CHUNK, smem + AT_CHUNK, w, lane);

  const int dv = c < 18 ? c : 18;
  const int koff = KV_KP + h * 512 + c * 16;
  const int toff = KV_KT + c * 4;
  const int voff = KV_V + h * 304 + dv * 16;

  f32x16 negm, O;
  u32x4 ktA = u32x4{0u, 0u, 0u, 0u}, ktB = u32x4{0u, 0u, 0u, 0u};
  {
    const bf16x8 kf = *(const bf16x8*)(smem + koff);
    ktA[0] = *(const unsigned*)(smem + toff);
    f32x16 z;
#pragma unroll
    for (int i = 0; i < 16; ++i) z[i] = 0.f;
    f32x16 S = MFMA32(kf, q0, z);
    S = MFMA32(__builtin_bit_cast(bf16x8, ktA), q1, S);
    float mx = max16(S);
    mx = fmaxf(mx, __shfl_xor(mx, 32));
#pragma unroll
    for (int i = 0; i < 16; ++i) { negm[i] = -mx; O[i] = 0.f; }
  }
  for (int ch = 0; ch < 4; ++ch) {
    const char* kb = smem + (ch & 1) * AT_CHUNK + koff;
    const char* tb = smem + (ch & 1) * AT_CHUNK + toff;
    const char* vb = smem + (ch & 1) * AT_CHUNK + voff;
    f32x16 SA, SB;
    {
      const bf16x8 kf = *(const bf16x8*)(kb);
      ktA[0] = *(const unsigned*)(tb);
      SA = MFMA32(kf, q0, negm);
      SA = MFMA32(__builtin_bit_cast(bf16x8, ktA), q1, SA);
    }
#pragma unroll
    for (int t2 = 0; t2 < 8; ++t2) {
      {
        const int t = 2 * t2;
        const bf16x8 vf0 = *(const bf16x8*)(vb + t * KV_TILE);
        const bf16x8 vf1 = *(const bf16x8*)(vb + t * KV_TILE + 608);
        const bf16x8 kf = *(const bf16x8*)(kb + (t + 1) * KV_TILE);
        ktB[0] = *(const unsigned*)(tb + (t + 1) * KV_TILE);
        SB = MFMA32(kf, q0, negm);
        const bf16x8 p0 = pack8(SA, 0);
        SB = MFMA32(__builtin_bit_cast(bf16x8, ktB), q1, SB);
        O = MFMA32(vf0, p0, O);
        O = MFMA32(vf1, pack8(SA, 8), O);
      }
      {
        const int t = 2 * t2 + 1;
        const bf16x8 vf0 = *(const bf16x8*)(vb + t * KV_TILE);
        const bf16x8 vf1 = *(const bf16x8*)(vb + t * KV_TILE + 608);
        if (t < 15) {
          const bf16x8 kf = *(const bf16x8*)(kb + (t + 1) * KV_TILE);
          ktA[0] = *(const unsigned*)(tb + (t + 1) * KV_TILE);
          SA = MFMA32(kf, q0, negm);
        }
        const bf16x8 p0 = pack8(SB, 0);
        if (t < 15) SA = MFMA32(__builtin_bit_cast(bf16x8, ktA), q1, SA);
        O = MFMA32(vf0, p0, O);
        O = MFMA32(vf1, pack8(SB, 8), O);
      }
    }
    if (ch < 3) {
      wait_vm0();
      __syncthreads();
      if (ch < 2) at_issue_chunk(kvsrc + (ch + 2) * AT_CHUNK, smem + (ch & 1) * AT_CHUNK, w, lane);
    }
  }
  {
    bool ok = true;
#pragma unroll
    for (int i = 0; i < 11; ++i) ok = ok && (fabsf(O[i]) < 1.0e30f);
    ok = ok && (h != 0 || O[10] < 1.8e19f);
    if (!__all(ok)) O = attn_safe_pass(kvsrc, q0, q1, koff, toff, voff);
  }
  {
    const float l = __shfl(O[10], c);
    const float inv = 1.f / l;
    unsigned* op = (unsigned*)((u16*)(ws + WS_O) + (size_t)token * DM + c * DH);
    op[2 * h + 0] = pack2(O[0] * inv, O[1] * inv);
    op[2 * h + 1] = pack2(O[2] * inv, O[3] * inv);
    op[4 + 2 * h + 0] = pack2(O[4] * inv, O[5] * inv);
    op[4 + 2 * h + 1] = pack2(O[6] * inv, O[7] * inv);
    if (h == 0) op[8] = pack2(O[8] * inv, O[9] * inv);
  }
}

constexpr int PC_STRIDE = 224;
constexpr int PC_LDS = 576 * PC_STRIDE;
__global__ void __launch_bounds__(256) out_kernel(const u16* __restrict__ x, const u16* __restrict__ wo,
                                                  const u16* __restrict__ dskip, const char* __restrict__ ws,
                                                  u16* __restrict__ out) {
  extern __shared__ __attribute__((aligned(16))) char smem[];
  const int tid = threadIdx.x, lane = tid & 63, w = tid >> 6;
  const int cb = blockIdx.x % 6, rb = blockIdx.x / 6;
  const int fr = lane & 15, g = lane >> 4;
  const int token = rb * 64 + w * 16 + fr;

  bf16x8 of[18];
  {
    const u16* op = (const u16*)(ws + WS_O) + (size_t)token * DM + 8 * g;
#pragma unroll
    for (int s = 0; s < 18; ++s) of[s] = *(const bf16x8*)(op + 32 * s);
  }
  for (int it = w; it < 126; it += 4) {
    const int ci = it * 64 + lane;
    const int row = ci / 14;
    int ch = ci - row * 14;
    ch = ch < 12 ? ch : 0;
    glds16(wo + (size_t)rho(row) * DM + cb * 96 + ch * 8, smem + it * 1024);
  }
  wait_vm0();
  __syncthreads();
  f32x4 acc[6];
#pragma unroll
  for (int c = 0; c < 6; ++c) acc[c] = f32x4{0.f, 0.f, 0.f, 0.f};
  const int q4 = (lane & 15) >> 2, p4 = lane & 3;
#pragma unroll
  for (int s = 0; s < 18; ++s) {
    const char* b0 = smem + rho(32 * s + 8 * g + q4) * PC_STRIDE + 8 * p4;
    const char* b1 = smem + rho(32 * s + 8 * g + 4 + q4) * PC_STRIDE + 8 * p4;
#pragma unroll
    for (int c = 0; c < 6; ++c) {
      const s16x4 lo = tr_read(b0 + 32 * c), hi = tr_read(b1 + 32 * c);
      const bf16x8 wf = __builtin_shufflevector(lo, hi, 0, 1, 2, 3, 4, 5, 6, 7);
      acc[c] = MFMA16(wf, of[s], acc[c]);
    }
  }
  const float gg = ((const float*)(ws + WS_G))[token];
  const float og = bfr(1.f - gg);
  const u16* Y = (const u16*)(ws + WS_Y);
#pragma unroll
  for (int c = 0; c < 6; ++c) {
    const int col = cb * 96 + 16 * c + 4 * g;
    const s16x4 uv = *(const s16x4*)(x + (size_t)token * DM + col);
    const s16x4 dv = *(const s16x4*)(dskip + col);
    float r[4];
#pragma unroll
    for (int i = 0; i < 4; ++i) {
      const float y = bf2f(Y[(size_t)(col + i) * L_SEQ + token]);
      const float a = bfr(y + bfr(bf2f((u16)uv[i]) * bf2f((u16)dv[i])));
      const float b = bfr(acc[c][i]);
      r[i] = bfr(gg * a) + bfr(og * b);
    }
    u32x2 v;
    v[0] = pack2(r[0], r[1]);
    v[1] = pack2(r[2], r[3]);
    *(u32x2*)(out + (size_t)token * DM + col) = v;
  }
}

extern "C" void kernel_launch(void* const* d_in, const int* in_sizes, int n_in, void* d_out, int out_size,
                              void* d_ws, size_t ws_size, hipStream_t stream) {
  const u16* x = (const u16*)d_in[0];
  const u16* logA = (const u16*)d_in[1];
  const u16* Aim = (const u16*)d_in[2];
  const u16* Cre = (const u16*)d_in[3];
  const u16* Cim = (const u16*)d_in[4];
  const u16* logdt = (const u16*)d_in[5];
  const u16* dskip = (const u16*)d_in[6];
  const u16* wq = (const u16*)d_in[8];
  const u16* wk = (const u16*)d_in[9];
  const u16* wv = (const u16*)d_in[10];
  const u16* wo = (const u16*)d_in[11];
  const u16* gw = (const u16*)d_in[12];
  char* ws = (char*)d_ws;
  (void)hipFuncSetAttribute((const void*)proj_kernel, hipFuncAttributeMaxDynamicSharedMemorySize, PA_LDS);
  (void)hipFuncSetAttribute((const void*)ssm_kernel, hipFuncAttributeMaxDynamicSharedMemorySize, SS_LDS);
  (void)hipFuncSetAttribute((const void*)attn_kernel, hipFuncAttributeMaxDynamicSharedMemorySize, AT_LDS);
  (void)hipFuncSetAttribute((const void*)out_kernel, hipFuncAttributeMaxDynamicSharedMemorySize, PC_LDS);
  proj_kernel<<<256, 256, PA_LDS, stream>>>(x, wq, wk, wv, gw, ws);
  ssm_kernel<<<DM, 256, SS_LDS, stream>>>(logA, Aim, Cre, Cim, logdt, ws);
  attn_kernel<<<256, 512, AT_LDS, stream>>>(ws);
  out_kernel<<<192, 256, PC_LDS, stream>>>(x, wo, dskip, ws, (u16*)d_out);
}
```

```cpp
#include <hip/hip_runtime.h>
#include <hip/hip_bf16.h>
#include <stdint.h>


constexpr int L_SEQ = 2048;
constexpr int DM = 576;
constexpr int DH = 18;
constexpr int NST = 128;

typedef __attribute__((ext_vector_type(8))) short bf16x8;
typedef __attribute__((ext_vector_type(4))) short s16x4;
typedef __attribute__((ext_vector_type(4))) float f32x4;
typedef __attribute__((ext_vector_type(16))) float f32x16;
typedef __attribute__((ext_vector_type(4))) unsigned u32x4;
typedef __attribute__((ext_vector_type(2))) unsigned u32x2;
typedef __attribute__((ext_vector_type(4))) short v4i16_t;
typedef unsigned short u16;

#define DI __device__ __forceinline__
#define MFMA32(a, b, c) __builtin_amdgcn_mfma_f32_32x32x16_bf16((a), (b), (c), 0, 0, 0)
typedef __attribute__((ext_vector_type(8))) _Float16 f16x8;
typedef __attribute__((ext_vector_type(2))) _Float16 f16x2;
#define MFMA32H(a, b, c) __builtin_amdgcn_mfma_f32_32x32x16_f16((a), (b), (c), 0, 0, 0)

constexpr size_t WS_Q = 0;
constexpr size_t WS_O = WS_Q + (size_t)L_SEQ * DM * 2;
constexpr size_t WS_UT = WS_O + (size_t)L_SEQ * DM * 2;
constexpr size_t WS_Y = WS_UT + (size_t)L_SEQ * DM * 2;
constexpr size_t WS_KV = WS_Y + (size_t)L_SEQ * DM * 2;
constexpr int KV_KP = 0;
constexpr int KV_KT = 1024;
constexpr int KV_V = 1152;
constexpr int KV_TILE = 2432;
constexpr int KV_BYTES = 64 * KV_TILE;
constexpr size_t WS_G = WS_KV + KV_BYTES;
constexpr size_t WS_R = WS_G + L_SEQ * 4;
constexpr int R_ROW = 4096 + 256;
constexpr size_t WS_UM = WS_R + (size_t)DM * R_ROW;
constexpr size_t WS_CNT = (WS_UM + 16 * DM * 4 + 8191) / 4096 * 4096;
constexpr int CV_SPLIT = 256;
constexpr size_t WS_Y2 = ((WS_CNT + 4096 + 65535) / 65536 + 1) * 65536;

DI float bf2f(u16 v) { return __uint_as_float(((unsigned)v) << 16); }
DI u16 f2bf(float x) {
  __hip_bfloat16 b = __float2bfloat16(x);
  return __builtin_bit_cast(u16, b);
}
DI float bfr(float x) { return bf2f(f2bf(x)); }
typedef __attribute__((ext_vector_type(2))) float f32x2;
typedef __attribute__((ext_vector_type(2))) __bf16 bf16x2_t;
DI unsigned cvtpk(float lo, float hi) {
  const f32x2 v = f32x2{lo, hi};
  return __builtin_bit_cast(unsigned, __builtin_convertvector(v, bf16x2_t));
}
DI unsigned pack2(float lo, float hi) { return cvtpk(lo, hi); }
DI s16x4 tr_read(const char* p) {
  return __builtin_bit_cast(s16x4, __builtin_amdgcn_ds_read_tr16_b64_v4i16(
                                       (__attribute__((address_space(3))) v4i16_t*)p));
}
DI void glds16(const void* gsrc, char* lds_wave_base) {
  __builtin_amdgcn_global_load_lds((const __attribute__((address_space(1))) void*)gsrc,
                                   (__attribute__((address_space(3))) void*)lds_wave_base, 16, 0, 0);
}
DI float pow2_scale(float mx) {
  const int e = (int)((__float_as_uint(mx) >> 23) & 0xff);
  if (e == 0xff || mx == 0.f) return 1.f;
  int se = 127 + 14 - (e - 127);
  se = se > 254 ? 254 : (se < 1 ? 1 : se);
  return __uint_as_float((unsigned)se << 23);
}
DI unsigned cvtpk_h(float lo, float hi) {
  const f16x2 v = f16x2{(_Float16)lo, (_Float16)hi};
  return __builtin_bit_cast(unsigned, v);
}
DI float mul1(float a, float b) {
  float r;
  asm("s_nop 0\n\tv_mul_f32 %0, %1, %2" : "=v"(r) : "v"(a), "v"(b));
  return r;
}
DI float wave_sum(float v) {
#define DPP_ADD(ctrl, rmask) \
  v += __int_as_float(__builtin_amdgcn_update_dpp(0, __float_as_int(v), ctrl, rmask, 0xf, false))
  DPP_ADD(0x111, 0xf);
  DPP_ADD(0x112, 0xf);
  DPP_ADD(0x114, 0xf);
  DPP_ADD(0x118, 0xf);
  DPP_ADD(0x142, 0xa);
  DPP_ADD(0x143, 0xc);
#undef DPP_ADD
  return __int_as_float(__builtin_amdgcn_readlane(__float_as_int(v), 63));
}
DI void wait_lds0() { asm volatile("s_waitcnt lgkmcnt(0)" ::: "memory"); }
DI void wait_vm0() { asm volatile("s_waitcnt vmcnt(0)" ::: "memory"); }

constexpr int GM_STAGE = 24576;
constexpr int GM_LDS = 3 * GM_STAGE;
DI void dma16(const void* gsrc, unsigned lds_addr) {
  unsigned keep;
  asm volatile("s_mov_b32 %0, m0\n\ts_mov_b32 m0, %2\n\ts_nop 0\n\tglobal_load_lds_dwordx4 %1, off\n\ts_mov_b32 m0, %0"
               : "=&s"(keep) : "v"(gsrc), "s"(lds_addr) : "memory");
}
template <bool WREG>
DI f32x16 gemm_tile(const u16* __restrict__ A, const u16* __restrict__ W, const unsigned* wsrc, const u16* gsrc,
                    char* smem) {
  const int tid = threadIdx.x, lane = tid & 63;
  const int w = __builtin_amdgcn_readfirstlane(tid >> 6);
  const unsigned lbase = (unsigned)(size_t)smem;
  const int pr = lane >> 3, ps = lane & 7;
  const u16* a0 = A + (size_t)(8 * w + pr) * DM + ((ps ^ pr) * 8);
  const u16* a1 = a0 + (size_t)64 * DM;
  const int wk = 8 * w + pr;
  const u16* w0 = W + (size_t)wk * DM + ((ps ^ (((wk >> 1) & 1) << 2)) * 8);
  u32x4 wreg[9];
  unsigned greg[9];
  auto issue_a = [&](int c) {
    const unsigned sb = lbase + (c % 3) * GM_STAGE;
    dma16(a0 + 64 * c, sb + w * 1024);
    dma16(a1 + 64 * c, sb + (w + 8) * 1024);
    if (!WREG) dma16(w0 + (size_t)64 * c * DM, sb + 16384 + w * 1024);
  };
  auto load_w = [&](int c) {
    asm volatile("global_load_ushort %0, %2, off\n\tglobal_load_dwordx4 %1, %3, off"
                 : "=&v"(greg[c]), "=&v"(wreg[c]) : "v"(gsrc + 64 * c), "v"(wsrc + 64 * 9 * c) : "memory");
  };
  auto write_w = [&](int c) {
    asm volatile("" : "+v"(wreg[c]), "+v"(greg[c]));
    u32x4 v = wreg[c];
    if (ps == 2 || ps == 5) v = u32x4{v[3], 0u, 0u, 0u};
    if (ps >= 6) v = u32x4{ps == 6 ? greg[c] : 0u, 0u, 0u, 0u};
    *(u32x4*)(smem + (c % 3) * GM_STAGE + 16384 + wk * 128 + ((ps ^ (((wk >> 1) & 1) << 2)) * 16)) = v;
  };
  const int m = lane & 31, h = lane >> 5;
  const int arow = 32 * (w & 3) + m;
  const int aoff = arow * 128;
  const int q4 = (lane & 15) >> 2, p4 = lane & 3, blk = (lane >> 4) & 1;
  const int wcol = 2 * (w >> 2) + blk;
  const int woff = 16384 + (8 * h + q4) * 128 + 8 * p4;
  f32x16 acc;
#pragma unroll
  for (int i = 0; i < 16; ++i) acc[i] = 0.f;
  if (!WREG) {
    issue_a(0);
    issue_a(1);
  } else {
    load_w(0);
    load_w(1);
    issue_a(0);
    load_w(2);
    issue_a(1);
#pragma unroll
    for (int c = 3; c < 9; ++c) load_w(c);
    asm volatile("s_waitcnt vmcnt(18)" ::: "memory");
    write_w(0);
    write_w(1);
  }
#pragma unroll
  for (int c = 0; c < 9; ++c) {
    if (c < 8) {
      if (WREG && c == 0) asm volatile("s_waitcnt vmcnt(14) lgkmcnt(0)\n\ts_barrier" ::: "memory");
      else if (WREG && c == 1) asm volatile("s_waitcnt vmcnt(12) lgkmcnt(0)\n\ts_barrier" ::: "memory");
      else if (WREG) asm volatile("s_waitcnt vmcnt(2) lgkmcnt(0)\n\ts_barrier" ::: "memory");
      else asm volatile("s_waitcnt vmcnt(3) lgkmcnt(0)\n\ts_barrier" ::: "memory");
    } else {
      asm volatile("s_waitcnt vmcnt(0) lgkmcnt(0)\n\ts_barrier" ::: "memory");
    }
    if (c + 2 < 9) {
      issue_a(c + 2);
      if (WREG) write_w(c + 2);
    }
    const char* st = smem + (c % 3) * GM_STAGE;
#pragma unroll
    for (int ks = 0; ks < 4; ++ks) {
      const bf16x8 af = *(const bf16x8*)(st + aoff + (((2 * ks + h) ^ (arow & 7)) * 16));
      const s16x4 lo = tr_read(st + woff + (16 * ks) * 128 + ((wcol ^ (((q4 >> 1) & 1) << 1)) * 32));
      const s16x4 hi = tr_read(st + woff + (16 * ks + 4) * 128 + ((wcol ^ (((q4 >> 1) & 1) << 1)) * 32));
      const bf16x8 wf = __builtin_shufflevector(lo, hi, 0, 1, 2, 3, 4, 5, 6, 7);
      acc = MFMA32(wf, af, acc);
    }
  }
  return acc;
}

constexpr int KG_ROW = 528;
constexpr int KG_XH = 0;
constexpr int KG_YH = KG_XH + 21 * KG_ROW;
constexpr int KG_KP = 28032;
constexpr int KG_HALF = KG_KP + 2052 * 4;
constexpr int KG_LDS = 2 * KG_HALF;

struct cf { float re, im; };
DI cf cmul(cf a, cf b) {
  float t0, t1, re, im;
  asm("v_mul_f32 %0, %1, %2" : "=v"(t0) : "v"(a.im), "v"(b.im));
  asm("v_mul_f32 %0, %1, %2" : "=v"(t1) : "v"(a.im), "v"(b.re));
  asm("v_fma_f32 %0, %1, %2, -%3" : "=v"(re) : "v"(a.re), "v"(b.re), "v"(t0));
  asm("v_fma_f32 %0, %1, %2, %3" : "=v"(im) : "v"(a.re), "v"(b.im), "v"(t1));
  return cf{re, im};
}
DI cf cexp_pos(float ar, float ai, float pos) {
  const float e = __expf(ar * pos);
  const float rev = (ai * pos) * 0.15915494309189535f;
  const float fr = rev - floorf(rev);
  return cf{mul1(e, __builtin_amdgcn_cosf(fr)), mul1(e, __builtin_amdgcn_sinf(fr))};
}
DI void put_x(char* smem, int i, int n, cf t) { *(unsigned*)(smem + KG_XH + i * KG_ROW + 4 * n) = cvtpk_h(t.re, -t.im); }

DI void kgen_body(int pair, char* smem_all, const u16* __restrict__ logA, const u16* __restrict__ Aim,
                  const u16* __restrict__ Cre, const u16* __restrict__ Cim, const u16* __restrict__ logdt,
                  char* __restrict__ ws) {
  const int lane = threadIdx.x & 63;
  const int wall = __builtin_amdgcn_readfirstlane(threadIdx.x >> 6);
  const int w = wall & 3, tid = threadIdx.x & 255;
  const int d = 2 * pair + (wall >> 2);
  char* smem = smem_all + (wall >> 2) * KG_HALF;
  char* XA = smem;
  float sx;
  {
    const int n = tid & 127, part = w >> 1;
    const float dt = bfr(__expf(bf2f(logdt[d])));
    const float ar = -bfr(__expf(bf2f(logA[d * NST + n]))) * dt;
    const float ai = bf2f(Aim[d * NST + n]) * dt;
    const float cs = wave_sum(fabsf(bf2f(Cre[d * NST + lane])) + fabsf(bf2f(Cim[d * NST + lane])) +
                              fabsf(bf2f(Cre[d * NST + 64 + lane])) + fabsf(bf2f(Cim[d * NST + 64 + lane])));
    sx = pow2_scale(2.f * cs * dt);
    if (part == 0) {
      const cf e8 = cexp_pos(ar, ai, 8.f);
#pragma unroll
      for (int jb = 0; jb < 4; ++jb) {
        cf cur = cexp_pos(ar, ai, 64.f * jb);
#pragma unroll
        for (int jj = 0; jj < 8; ++jj) {
          *(unsigned*)(smem + KG_YH + (jb * 8 + jj) * KG_ROW + 4 * n) = cvtpk_h(cur.re, cur.im);
          cur = cmul(cur, e8);
        }
      }
    } else {
      const cf c2 = cf{2.f * bf2f(Cre[d * NST + n]) * dt * sx, 2.f * bf2f(Cim[d * NST + n]) * dt * sx};
      const cf e1 = cexp_pos(ar, ai, 1.f);
      const cf e2 = cexp_pos(ar, ai, 2.f);
      const cf e4 = cexp_pos(ar, ai, 4.f);
      const cf e6 = cmul(e2, e4);
      cf cur = c2;
#pragma unroll
      for (int i = 0; i < 8; ++i) {
        put_x(XA, i, n, cur);
        cur = cmul(cur, e1);
      }
      cf t = cmul(c2, cexp_pos(ar, ai, 256.f));
      put_x(XA, 8, n, t);
      put_x(XA, 9, n, cmul(t, e2));
      put_x(XA, 10, n, cmul(t, e4));
      put_x(XA, 11, n, cmul(t, e6));
      t = cmul(c2, cexp_pos(ar, ai, 512.f));
      put_x(XA, 12, n, t);
      put_x(XA, 13, n, cmul(t, e4));
      t = cmul(c2, cexp_pos(ar, ai, 768.f));
      put_x(XA, 14, n, t);
      put_x(XA, 15, n, cmul(t, e4));
#pragma unroll
      for (int i = 0; i < 5; ++i) put_x(XA, 16 + i, n, cmul(c2, cexp_pos(ar, ai, 1024.f + 256.f * i)));
    }
  }
  __syncthreads();
  {
    const int ti = w & 1, tj = w >> 1;
    const int r = lane & 15, kk = lane >> 4;
    const int ra = (16 * ti + r) < 21 ? (16 * ti + r) : 20;
    const char* ap = smem + KG_XH + ra * KG_ROW + 16 * kk;
    const char* bp = smem + KG_YH + (16 * tj + r) * KG_ROW + 16 * kk;
    f16x8 ah[8], bh[8];
#pragma unroll
    for (int qq = 0; qq < 8; ++qq) {
      ah[qq] = *(const f16x8*)(ap + 64 * qq);
      bh[qq] = *(const f16x8*)(bp + 64 * qq);
    }
    f32x4 acc0 = f32x4{0.f, 0.f, 0.f, 0.f}, acc1 = f32x4{0.f, 0.f, 0.f, 0.f};
#pragma unroll
    for (int qq = 0; qq < 8; qq += 2) {
      acc0 = __builtin_amdgcn_mfma_f32_16x16x32_f16(ah[qq], bh[qq], acc0, 0, 0, 0);
      acc1 = __builtin_amdgcn_mfma_f32_16x16x32_f16(ah[qq + 1], bh[qq + 1], acc1, 0, 0, 0);
    }
    float* kp = (float*)(smem + KG_KP);
    const int j = 16 * tj + r;
#pragma unroll
    for (int e = 0; e < 4; ++e) {
      const int i = 16 * ti + 4 * kk + e;
      const int base = i < 8 ? i : i < 12 ? 256 + 2 * (i - 8) : i < 16 ? 512 + 256 * ((i - 12) >> 1) + 4 * ((i - 12) & 1) : 1024 + 256 * (i - 16);
      if (i < 20 || (i == 20 && j == 0)) kp[base + 8 * j] = acc0[e] + acc1[e];
    }
  }
  __syncthreads();
  {
    const float* kp = (const float*)(smem + KG_KP);
    float kv[8];
#pragma unroll
    for (int e = 0; e < 8; ++e) kv[e] = kp[(int)bfr((float)(2047 - (8 * tid + e)))];
    u32x4 vh;
#pragma unroll
    for (int e = 0; e < 4; ++e) vh[e] = cvtpk_h(kv[2 * e], kv[2 * e + 1]);
    *(u32x4*)(ws + WS_R + (size_t)d * R_ROW + tid * 16) = vh;
    if (tid == 0) *(float*)(ws + WS_R + (size_t)d * R_ROW + 4096) = 1.f / sx;
  }
}

DI void transpose_body(int blk, const u16* __restrict__ x, char* smem, char* __restrict__ ws) {
  const int tid = threadIdx.x;
  const int rg = blk & 7, cb = blk >> 3;
  const u16* xa = x + (size_t)rg * 256 * DM + cb * 72;
  bf16x8 tv[5];
#pragma unroll
  for (int i = 0; i < 5; ++i) {
    const int ci = tid + 512 * i < 2304 ? tid + 512 * i : tid;
    const int r = ci / 9, cq = ci - 9 * r;
    tv[i] = *(const bf16x8*)(xa + (size_t)r * DM + cq * 8);
  }
  u16* tl = (u16*)smem;
#pragma unroll
  for (int i = 0; i < 5; ++i) {
    const int ci = tid + 512 * i < 2304 ? tid + 512 * i : tid;
    const int r = ci / 9, cq = ci - 9 * r;
#pragma unroll
    for (int j = 0; j < 8; ++j) tl[(cq * 8 + j) * 264 + r] = (u16)tv[i][j];
  }
  __syncthreads();
#pragma unroll
  for (int i = 0; i < 5; ++i) {
    const int ci = tid + 512 * i < 2304 ? tid + 512 * i : tid;
    const int ch = ci >> 5, p = ci & 31;
    const u32x4 v = *(const u32x4*)(tl + ch * 264 + p * 8);
    *(u32x4*)((u16*)(ws + WS_UT) + (size_t)(cb * 72 + ch) * L_SEQ + rg * 256 + p * 8) = v;
    unsigned mx = 0u;
#pragma unroll
    for (int e = 0; e < 4; ++e) mx = max(mx, max(v[e] & 0x7fffu, (v[e] >> 16) & 0x7fffu));
#pragma unroll
    for (int o = 16; o > 0; o >>= 1) mx = max(mx, (unsigned)__shfl_xor((int)mx, o));
    if (p == 0) ((unsigned*)(ws + WS_UM))[(rg * 8 + cb) * 128 + ch] = mx;
  }
}

constexpr int PA_LDS = GM_LDS > KG_LDS ? GM_LDS : KG_LDS;
__global__ void __launch_bounds__(512) proj_kernel(const u16* __restrict__ x, const u16* __restrict__ wq,
                                                   const u16* __restrict__ wk, const u16* __restrict__ wv,
                                                   const u16* __restrict__ gw, const u16* __restrict__ logA,
                                                   const u16* __restrict__ Aim, const u16* __restrict__ Cre,
                                                   const u16* __restrict__ Cim, const u16* __restrict__ logdt,
                                                   char* __restrict__ ws) {
  extern __shared__ __attribute__((aligned(16))) char smem[];
  if (blockIdx.x >= 224) {
    kgen_body(blockIdx.x - 224, smem, logA, Aim, Cre, Cim, logdt, ws);
    return;
  }
  if (blockIdx.x >= 160) {
    if (blockIdx.x == 160 && threadIdx.x < 9) ((unsigned*)(ws + WS_CNT))[threadIdx.x * 32] = 0u;
    transpose_body(blockIdx.x - 160, x, smem, ws);
    return;
  }
  const int tid = threadIdx.x, lane = tid & 63, w = tid >> 6;
  const int j8 = blockIdx.x >> 3;
  const int rb = (blockIdx.x & 7) * 2 + (j8 & 1), cb = j8 >> 1;
  const int m = lane & 31, h = lane >> 5;
  const int token = rb * 128 + 32 * (w & 3) + m;
  const u16* xa = x + (size_t)rb * 128 * DM;
  if (cb < 9) {
    const f32x16 acc = gemm_tile<false>(xa, wq + cb * 64, nullptr, nullptr, smem);
    const float qscale = 0.23570226039551584f * 1.4426950408889634f;
    u16* Q = (u16*)(ws + WS_Q) + (size_t)token * DM + cb * 64 + 32 * (w >> 2) + 4 * h;
#pragma unroll
    for (int g = 0; g < 4; ++g) {
      u32x2 v;
      v[0] = pack2(acc[4 * g] * qscale, acc[4 * g + 1] * qscale);
      v[1] = pack2(acc[4 * g + 2] * qscale, acc[4 * g + 3] * qscale);
      *(u32x2*)(Q + 8 * g) = v;
    }
  } else {
    const int ps = lane & 7;
    const int k0 = 8 * w + (lane >> 3);
    const int pp = ps < 3 ? ps : ps < 6 ? ps - 3 : 0;
    const unsigned* src = (const unsigned*)(ps < 3 ? wk : wv) + k0 * 9 + (pp < 2 ? 4 * pp : 5);
    const f32x16 acc = gemm_tile<true>(xa, wq, src, gw + k0, smem);
    __syncthreads();
    char* kt = smem + (w & 3) * KV_TILE;
    const int k16 = m & 15;
    const int vofs = KV_V + ((m >> 4) * 2 + ((k16 >> 2) & 1)) * 320 + ((k16 & 3) + 4 * (k16 >> 3)) * 2;
    char* vp = kt + vofs + 4 * h * 16;
    if (__builtin_amdgcn_readfirstlane(w) < 4) {
#pragma unroll
      for (int g = 0; g < 2; ++g)
        *(u32x2*)(kt + KV_KP + g * 512 + m * 16 + 8 * h) =
            u32x2{pack2(acc[4 * g], acc[4 * g + 1]), pack2(acc[4 * g + 2], acc[4 * g + 3])};
      *(unsigned*)(h == 0 ? kt + KV_KT + m * 4 : smem + 4 * KV_TILE + tid * 4) = pack2(acc[8], acc[9]);
#pragma unroll
      for (int i = 0; i < 4; ++i) *(u16*)(vp + i * 16) = f2bf(acc[12 + i]);
    } else {
#pragma unroll
      for (int i = 0; i < 4; ++i) *(u16*)(vp + (8 + i) * 16) = f2bf(acc[i]);
      char* vq = kt + vofs + (16 + 2 * h) * 16;
      *(u16*)(vq) = h == 0 ? f2bf(acc[4]) : (u16)0x3F80;
      *(u16*)(vq + 16) = h == 0 ? f2bf(acc[5]) : (u16)0;
      if (h == 0) {
        const float lg = bfr(acc[8]);
        ((float*)(ws + WS_G))[token] = bfr(1.f / (1.f + __expf(-lg)));
      }
    }
    __syncthreads();
    char* dst = ws + WS_KV + (size_t)rb * 4 * KV_TILE;
    for (int i = tid; i < 4 * KV_TILE / 16; i += 512) *(u32x4*)(dst + i * 16) = *(const u32x4*)(smem + i * 16);
  }
}

constexpr int CV_UC = 0;
constexpr int CV_UCS = 2064 * 2 + 16;
constexpr int CV_Z0 = 2055;
constexpr int CV_RH = 33280;
constexpr int CV_RED = 0;
constexpr int CV_LDS = 65536;
static_assert(8 * CV_UCS <= CV_RH, "copies overlap the kernel plane");

DI u32x4 window8(const unsigned (&dw)[8], int s) {
  u32x4 v;
#pragma unroll
  for (int e = 0; e < 4; ++e) {
    const int b = (s >> 1) + e;
    v[e] = (s & 1) ? __builtin_amdgcn_alignbyte(dw[b + 1], dw[b], 2) : dw[b];
  }
  return v;
}
DI unsigned bf2h_pair(unsigned v, float s) {
  return cvtpk_h(mul1(__uint_as_float(v << 16), s), mul1(__uint_as_float(v & 0xffff0000u), s));
}

template <bool PUBLISH>
DI void conv_body(int d, char* smem, char* __restrict__ ws) {
  const int tid = threadIdx.x, lane = tid & 63;
  const int w = __builtin_amdgcn_readfirstlane(tid >> 6);
  if (w < 4) glds16(ws + WS_R + (size_t)d * R_ROW + w * 1024 + lane * 16, smem + CV_RH + 16 + w * 1024);
  const u16* ut = (const u16*)(ws + WS_UT) + (size_t)d * L_SEQ;
  const int q = tid >> 1, rh = tid & 1;
  const u32x4 u0 = *(const u32x4*)(ut + q * 8);
  const u32x4 u1r = *(const u32x4*)(ut + (q < 255 ? q * 8 + 8 : 0));
  unsigned umx = ((const unsigned*)(ws + WS_UM))[((lane & 7) * 8 + d / 72) * 128 + d % 72];
  const float inv_sk = *(const float*)(ws + WS_R + (size_t)d * R_ROW + 4096);
  if (tid < 33) {
    *(u32x4*)(smem + CV_RH + (tid == 0 ? 0 : 256 + tid) * 16) = u32x4{0u, 0u, 0u, 0u};
    wait_lds0();
  }
#pragma unroll
  for (int o = 4; o > 0; o >>= 1) umx = max(umx, (unsigned)__shfl_xor((int)umx, o));
  const float su = pow2_scale(__uint_as_float(umx << 16));
  {
    char* uc = smem + CV_UC + 4 * rh * CV_UCS;
    const float s1 = q < 255 ? su : 0.f;
    const unsigned a2 = bf2h_pair(u0[2], su), a3 = bf2h_pair(u0[3], su);
    const unsigned b0 = bf2h_pair(u1r[0], s1), b1 = bf2h_pair(u1r[1], s1);
    const unsigned e0 = bf2h_pair(rh ? u1r[2] : u0[0], rh ? s1 : su), e1 = bf2h_pair(rh ? u1r[3] : u0[1], rh ? s1 : su);
    const unsigned ds[8] = {rh ? a2 : e0, rh ? a3 : e1, rh ? b0 : a2, rh ? b1 : a3, rh ? e0 : b0, rh ? e1 : b1, 0u, 0u};
#pragma unroll
    for (int r = 0; r < 4; ++r) *(u32x4*)(uc + r * CV_UCS + (q + 2) * 16) = window8(ds, r + 1);
    wait_lds0();
    if (q == 0) {
      const unsigned a0 = bf2h_pair(u0[0], su), a1 = bf2h_pair(u0[1], su);
      const unsigned dz[8] = {0u, 0u, rh ? a0 : 0u, rh ? a1 : 0u, rh ? a2 : a0, rh ? a3 : a1, 0u, 0u};
#pragma unroll
      for (int r = 0; r < 4; ++r) {
        *(u32x4*)(uc + r * CV_UCS + 16) = window8(dz, r + 1);
        *(u32x4*)(uc + r * CV_UCS) = u32x4{0u, 0u, 0u, 0u};
      }
      wait_lds0();
    }
  }
  wait_vm0();
  __syncthreads();
  f32x16 D0, D1;
#pragma unroll
  for (int i = 0; i < 16; ++i) { D0[i] = 0.f; D1[i] = 0.f; }
  const int c = lane & 31, h = lane >> 5;
  {
    const int r = c & 7, t = c >> 3;
    const int ulo = CV_UC + r * CV_UCS;
    const int ub = ulo + (256 * t + 8 * h - 16 * (w - 16)) * 2;
    const int ab = CV_RH + (CV_Z0 - 15 - 8 * c + 8 * h) * 2 - 32 * (w - 16 + 64);
    f16x8 b0[9], b1[9], a0[9], a1[9];
#pragma unroll
    for (int k = 0; k < 9; ++k) {
      if (k < 8 || w == 0) {
        const int uk = ub - 256 * k;
        b0[k] = *(const f16x8*)(smem + (uk < ulo ? ulo : uk));
        b1[k] = *(const f16x8*)(smem + uk + 2048);
        a0[k] = *(const f16x8*)(smem + ab - 256 * k + 2048);
        a1[k] = *(const f16x8*)(smem + ab - 256 * k);
        D0 = MFMA32H(b0[k], a0[k], D0);
        D1 = MFMA32H(b1[k], a0[k], D1);
        if (k > 0 || w != 0) D1 = MFMA32H(b0[k], a1[k], D1);
      }
    }
  }
  __syncthreads();
  {
    float* red = (float*)(smem + CV_RED) + w * 2048 + 8 * c + 4 * h;
#pragma unroll
    for (int g = 0; g < 4; ++g) {
      *(f32x4*)(red + 256 * g) = f32x4{D0[4 * g], D0[4 * g + 1], D0[4 * g + 2], D0[4 * g + 3]};
      *(f32x4*)(red + 1024 + 256 * g) = f32x4{D1[4 * g], D1[4 * g + 1], D1[4 * g + 2], D1[4 * g + 3]};
    }
  }
  __syncthreads();
  {
    const float* red = (const float*)(smem + CV_RED) + tid * 4;
    const f32x4 sum = ((*(const f32x4*)(red) + *(const f32x4*)(red + 2048)) + (*(const f32x4*)(red + 4096) + *(const f32x4*)(red + 6144))) +
                      ((*(const f32x4*)(red + 8192) + *(const f32x4*)(red + 10240)) + (*(const f32x4*)(red + 12288) + *(const f32x4*)(red + 14336)));
    const float fs = inv_sk * (1.f / su);
    const unsigned long long v = (unsigned long long)pack2(sum[0] * fs, sum[1] * fs) |
                                 ((unsigned long long)pack2(sum[2] * fs, sum[3] * fs) << 32);
    unsigned long long* yp =
        (unsigned long long*)((u16*)(ws + (PUBLISH ? WS_Y2 : WS_Y)) + (size_t)d * L_SEQ + tid * 4);
    if (!PUBLISH) {
      *yp = v;
    } else {
      __hip_atomic_store(yp, v, __ATOMIC_RELAXED, __HIP_MEMORY_SCOPE_SYSTEM);
      wait_vm0();
      __syncthreads();
      if (tid == 0)
        __hip_atomic_fetch_add((unsigned*)(ws + WS_CNT) + (d >> 6) * 32, 1u, __ATOMIC_RELAXED, __HIP_MEMORY_SCOPE_AGENT);
    }
  }
}

constexpr int AT_CHUNK = 16 * KV_TILE;
constexpr int AT_LDS = 2 * AT_CHUNK;
static_assert(CV_LDS <= AT_LDS, "conv blocks share the attention launch LDS size");
DI float max16(const f32x16& s) {
  float a = fmaxf(fmaxf(s[0], s[1]), s[2]);
  float b = fmaxf(fmaxf(s[3], s[4]), s[5]);
  float c = fmaxf(fmaxf(s[6], s[7]), s[8]);
  float d = fmaxf(fmaxf(s[9], s[10]), s[11]);
  a = fmaxf(fmaxf(a, s[12]), s[13]);
  b = fmaxf(fmaxf(b, s[14]), s[15]);
  return fmaxf(fmaxf(a, b), fmaxf(c, d));
}

#define ATTN_LOOP_ASM \
  "s_waitcnt lgkmcnt(0)\n" \
  "s_mov_b32 %1, m0\n" \
  "s_mov_b32 %2, 0\n" \
  "s_mov_b32 %3, 38912\n" \
  "v_mov_b32 v120, %7\n" \
  "v_mov_b32 v121, %8\n" \
  "v_mov_b32 v122, %9\n" \
  "v_add_u32 v123, 38912, %7\n" \
  "v_add_u32 v124, 38912, %8\n" \
  "v_add_u32 v125, 38912, %9\n" \
  "v_mov_b32 v61, %11\n" \
  "v_mov_b32 v62, %12\n" \
  "ds_read_b128 v[104:107], v120 offset:0\n" \
  "ds_read_b32 v108, v121 offset:0\n" \
  "ds_read_b128 v[112:115], v122 offset:0\n" \
  "v_mov_b32 v109, 0\n" \
  "v_mov_b32 v110, 0\n" \
  "v_mov_b32 v111, 0\n" \
  "v_mov_b32 v100, 0\n" \
  "v_mov_b32 v101, 0\n" \
  "v_mov_b32 v102, 0\n" \
  "v_mov_b32 v103, 0\n" \
  "v_mov_b32 v116, 0\n" \
  "v_mov_b32 v117, 0\n" \
  "v_mov_b32 v118, 0\n" \
  "v_mov_b32 v119, 0\n" \
  "s_waitcnt lgkmcnt(2)\n" \
  "v_mfma_f32_32x32x16_bf16 v[64:79], v[104:107], %5, %4\n" \
  "ds_read_b128 v[104:107], v120 offset:2432\n" \
  "s_waitcnt lgkmcnt(2)\n" \
  "v_mfma_f32_32x32x16_bf16 v[64:79], v[108:111], %6, v[64:79]\n" \
  "ds_read_b32 v108, v121 offset:2432\n" \
  "s_nop 7\n" \
  "s_waitcnt lgkmcnt(0)\n" \
  "1:\n" \
  "s_waitcnt lgkmcnt(3)\n" \
  "v_mfma_f32_32x32x16_bf16 v[80:95], v[104:107], %5, %4\n" \
  "ds_read_b128 v[104:107], v120 offset:4864\n" \
  "v_exp_f32 v64, v64\n" \
  "v_exp_f32 v65, v65\n" \
  "v_exp_f32 v66, v66\n" \
  "v_exp_f32 v67, v67\n" \
  "s_waitcnt lgkmcnt(3)\n" \
  "v_mfma_f32_32x32x16_bf16 %0, v[116:119], v[100:103], %0\n" \
  "ds_read_b128 v[116:119], v122 offset:640\n" \
  "v_exp_f32 v68, v68\n" \
  "v_exp_f32 v69, v69\n" \
  "v_exp_f32 v70, v70\n" \
  "v_exp_f32 v71, v71\n" \
  "s_waitcnt lgkmcnt(3)\n" \
  "v_mfma_f32_32x32x16_bf16 v[80:95], v[108:111], %6, v[80:95]\n" \
  "ds_read_b32 v108, v121 offset:4864\n" \
  "v_exp_f32 v72, v72\n" \
  "v_cvt_pk_bf16_f32 v96, v64, v65\n" \
  "v_exp_f32 v73, v73\n" \
  "v_cvt_pk_bf16_f32 v97, v66, v67\n" \
  "v_exp_f32 v74, v74\n" \
  "v_cvt_pk_bf16_f32 v98, v68, v69\n" \
  "v_exp_f32 v75, v75\n" \
  "v_cvt_pk_bf16_f32 v99, v70, v71\n" \
  "v_exp_f32 v76, v76\n" \
  "v_exp_f32 v77, v77\n" \
  "s_waitcnt lgkmcnt(3)\n" \
  "v_mfma_f32_32x32x16_bf16 %0, v[112:115], v[96:99], %0\n" \
  "ds_read_b128 v[112:115], v122 offset:2432\n" \
  "v_exp_f32 v78, v78\n" \
  "v_exp_f32 v79, v79\n" \
  "v_cvt_pk_bf16_f32 v100, v72, v73\n" \
  "v_cvt_pk_bf16_f32 v101, v74, v75\n" \
  "v_cvt_pk_bf16_f32 v102, v76, v77\n" \
  "v_cvt_pk_bf16_f32 v103, v78, v79\n" \
  "s_waitcnt lgkmcnt(3)\n" \
  "v_mfma_f32_32x32x16_bf16 v[64:79], v[104:107], %5, %4\n" \
  "ds_read_b128 v[104:107], v120 offset:7296\n" \
  "v_exp_f32 v80, v80\n" \
  "v_exp_f32 v81, v81\n" \
  "v_exp_f32 v82, v82\n" \
  "v_exp_f32 v83, v83\n" \
  "s_waitcnt lgkmcnt(3)\n" \
  "v_mfma_f32_32x32x16_bf16 %0, v[116:119], v[100:103], %0\n" \
  "ds_read_b128 v[116:119], v122 offset:3072\n" \
  "v_exp_f32 v84, v84\n" \
  "v_exp_f32 v85, v85\n" \
  "v_exp_f32 v86, v86\n" \
  "v_exp_f32 v87, v87\n" \
  "s_waitcnt lgkmcnt(3)\n" \
  "v_mfma_f32_32x32x16_bf16 v[64:79], v[108:111], %6, v[64:79]\n" \
  "ds_read_b32 v108, v121 offset:7296\n" \
  "v_exp_f32 v88, v88\n" \
  "v_cvt_pk_bf16_f32 v96, v80, v81\n" \
  "v_exp_f32 v89, v89\n" \
  "v_cvt_pk_bf16_f32 v97, v82, v83\n" \
  "v_exp_f32 v90, v90\n" \
  "v_cvt_pk_bf16_f32 v98, v84, v85\n" \
  "v_exp_f32 v91, v91\n" \
  "v_cvt_pk_bf16_f32 v99, v86, v87\n" \
  "v_exp_f32 v92, v92\n" \
  "v_exp_f32 v93, v93\n" \
  "s_waitcnt lgkmcnt(3)\n" \
  "v_mfma_f32_32x32x16_bf16 %0, v[112:115], v[96:99], %0\n" \
  "ds_read_b128 v[112:115], v122 offset:4864\n" \
  "v_exp_f32 v94, v94\n" \
  "v_exp_f32 v95, v95\n" \
  "v_cvt_pk_bf16_f32 v100, v88, v89\n" \
  "v_cvt_pk_bf16_f32 v101, v90, v91\n" \
  "v_cvt_pk_bf16_f32 v102, v92, v93\n" \
  "v_cvt_pk_bf16_f32 v103, v94, v95\n" \
  "s_waitcnt vmcnt(5)\n" \
  "s_barrier\n" \
  "s_cmp_eq_u32 %2, 0\n" \
  "s_cbranch_scc1 2f\n" \
  "s_cmp_eq_u32 %2, 3\n" \
  "s_cbranch_scc1 2f\n" \
  "s_add_u32 m0, %13, %3\n" \
  "v_add_u32 v63, 0, v61\n" \
  "global_load_lds_dwordx4 v63, %10\n" \
  "s_add_u32 m0, m0, 8192\n" \
  "v_add_u32 v63, 8192, v61\n" \
  "global_load_lds_dwordx4 v63, %10\n" \
  "s_add_u32 m0, m0, 8192\n" \
  "v_add_u32 v63, 16384, v61\n" \
  "global_load_lds_dwordx4 v63, %10\n" \
  "s_add_u32 m0, m0, 8192\n" \
  "v_add_u32 v63, 24576, v61\n" \
  "global_load_lds_dwordx4 v63, %10\n" \
  "s_add_u32 m0, %14, %3\n" \
  "s_nop 0\n" \
  "global_load_lds_dwordx4 v62, %10\n" \
  "v_add_u32 v61, 38912, v61\n" \
  "v_add_u32 v62, 38912, v62\n" \
  "2:\n" \
  "s_waitcnt lgkmcnt(3)\n" \
  "v_mfma_f32_32x32x16_bf16 v[80:95], v[104:107], %5, %4\n" \
  "ds_read_b128 v[104:107], v120 offset:9728\n" \
  "v_exp_f32 v64, v64\n" \
  "v_exp_f32 v65, v65\n" \
  "v_exp_f32 v66, v66\n" \
  "v_exp_f32 v67, v67\n" \
  "s_waitcnt lgkmcnt(3)\n" \
  "v_mfma_f32_32x32x16_bf16 %0, v[116:119], v[100:103], %0\n" \
  "ds_read_b128 v[116:119], v122 offset:5504\n" \
  "v_exp_f32 v68, v68\n" \
  "v_exp_f32 v69, v69\n" \
  "v_exp_f32 v70, v70\n" \
  "v_exp_f32 v71, v71\n" \
  "s_waitcnt lgkmcnt(3)\n" \
  "v_mfma_f32_32x32x16_bf16 v[80:95], v[108:111], %6, v[80:95]\n" \
  "ds_read_b32 v108, v121 offset:9728\n" \
  "v_exp_f32 v72, v72\n" \
  "v_cvt_pk_bf16_f32 v96, v64, v65\n" \
  "v_exp_f32 v73, v73\n" \
  "v_cvt_pk_bf16_f32 v97, v66, v67\n" \
  "v_exp_f32 v74, v74\n" \
  "v_cvt_pk_bf16_f32 v98, v68, v69\n" \
  "v_exp_f32 v75, v75\n" \
  "v_cvt_pk_bf16_f32 v99, v70, v71\n" \
  "v_exp_f32 v76, v76\n" \
  "v_exp_f32 v77, v77\n" \
  "s_waitcnt lgkmcnt(3)\n" \
  "v_mfma_f32_32x32x16_bf16 %0, v[112:115], v[96:99], %0\n" \
  "ds_read_b128 v[112:115], v122 offset:7296\n" \
  "v_exp_f32 v78, v78\n" \
  "v_exp_f32 v79, v79\n" \
  "v_cvt_pk_bf16_f32 v100, v72, v73\n" \
  "v_cvt_pk_bf16_f32 v101, v74, v75\n" \
  "v_cvt_pk_bf16_f32 v102, v76, v77\n" \
  "v_cvt_pk_bf16_f32 v103, v78, v79\n" \
  "s_waitcnt lgkmcnt(3)\n" \
  "v_mfma_f32_32x32x16_bf16 v[64:79], v[104:107], %5, %4\n" \
  "ds_read_b128 v[104:107], v120 offset:12160\n" \
  "v_exp_f32 v80, v80\n" \
  "v_exp_f32 v81, v81\n" \
  "v_exp_f32 v82, v82\n" \
  "v_exp_f32 v83, v83\n" \
  "s_waitcnt lgkmcnt(3)\n" \
  "v_mfma_f32_32x32x16_bf16 %0, v[116:119], v[100:103], %0\n" \
  "ds_read_b128 v[116:119], v122 offset:7936\n" \
  "v_exp_f32 v84, v84\n" \
  "v_exp_f32 v85, v85\n" \
  "v_exp_f32 v86, v86\n" \
  "v_exp_f32 v87, v87\n" \
  "s_waitcnt lgkmcnt(3)\n" \
  "v_mfma_f32_32x32x16_bf16 v[64:79], v[108:111], %6, v[64:79]\n" \
  "ds_read_b32 v108, v121 offset:12160\n" \
  "v_exp_f32 v88, v88\n" \
  "v_cvt_pk_bf16_f32 v96, v80, v81\n" \
  "v_exp_f32 v89, v89\n" \
  "v_cvt_pk_bf16_f32 v97, v82, v83\n" \
  "v_exp_f32 v90, v90\n" \
  "v_cvt_pk_bf16_f32 v98, v84, v85\n" \
  "v_exp_f32 v91, v91\n" \
  "v_cvt_pk_bf16_f32 v99, v86, v87\n" \
  "v_exp_f32 v92, v92\n" \
  "v_exp_f32 v93, v93\n" \
  "s_waitcnt lgkmcnt(3)\n" \
  "v_mfma_f32_32x32x16_bf16 %0, v[112:115], v[96:99], %0\n" \
  "ds_read_b128 v[112:115], v122 offset:9728\n" \
  "v_exp_f32 v94, v94\n" \
  "v_exp_f32 v95, v95\n" \
  "v_cvt_pk_bf16_f32 v100, v88, v89\n" \
  "v_cvt_pk_bf16_f32 v101, v90, v91\n" \
  "v_cvt_pk_bf16_f32 v102, v92, v93\n" \
  "v_cvt_pk_bf16_f32 v103, v94, v95\n" \
  "s_waitcnt lgkmcnt(3)\n" \
  "v_mfma_f32_32x32x16_bf16 v[80:95], v[104:107], %5, %4\n" \
  "ds_read_b128 v[104:107], v120 offset:14592\n" \
  "v_exp_f32 v64, v64\n" \
  "v_exp_f32 v65, v65\n" \
  "v_exp_f32 v66, v66\n" \
  "v_exp_f32 v67, v67\n" \
  "s_waitcnt lgkmcnt(3)\n" \
  "v_mfma_f32_32x32x16_bf16 %0, v[116:119], v[100:103], %0\n" \
  "ds_read_b128 v[116:119], v122 offset:10368\n" \
  "v_exp_f32 v68, v68\n" \
  "v_exp_f32 v69, v69\n" \
  "v_exp_f32 v70, v70\n" \
  "v_exp_f32 v71, v71\n" \
  "s_waitcnt lgkmcnt(3)\n" \
  "v_mfma_f32_32x32x16_bf16 v[80:95], v[108:111], %6, v[80:95]\n" \
  "ds_read_b32 v108, v121 offset:14592\n" \
  "v_exp_f32 v72, v72\n" \
  "v_cvt_pk_bf16_f32 v96, v64, v65\n" \
  "v_exp_f32 v73, v73\n" \
  "v_cvt_pk_bf16_f32 v97, v66, v67\n" \
  "v_exp_f32 v74, v74\n" \
  "v_cvt_pk_bf16_f32 v98, v68, v69\n" \
  "v_exp_f32 v75, v75\n" \
  "v_cvt_pk_bf16_f32 v99, v70, v71\n" \
  "v_exp_f32 v76, v76\n" \
  "v_exp_f32 v77, v77\n" \
  "s_waitcnt lgkmcnt(3)\n" \
  "v_mfma_f32_32x32x16_bf16 %0, v[112:115], v[96:99], %0\n" \
  "ds_read_b128 v[112:115], v122 offset:12160\n" \
  "v_exp_f32 v78, v78\n" \
  "v_exp_f32 v79, v79\n" \
  "v_cvt_pk_bf16_f32 v100, v72, v73\n" \
  "v_cvt_pk_bf16_f32 v101, v74, v75\n" \
  "v_cvt_pk_bf16_f32 v102, v76, v77\n" \
  "v_cvt_pk_bf16_f32 v103, v78, v79\n" \
  "s_waitcnt lgkmcnt(3)\n" \
  "v_mfma_f32_32x32x16_bf16 v[64:79], v[104:107], %5, %4\n" \
  "ds_read_b128 v[104:107], v120 offset:17024\n" \
  "v_exp_f32 v80, v80\n" \
  "v_exp_f32 v81, v81\n" \
  "v_exp_f32 v82, v82\n" \
  "v_exp_f32 v83, v83\n" \
  "s_waitcnt lgkmcnt(3)\n" \
  "v_mfma_f32_32x32x16_bf16 %0, v[116:119], v[100:103], %0\n" \
  "ds_read_b128 v[116:119], v122 offset:12800\n" \
  "v_exp_f32 v84, v84\n" \
  "v_exp_f32 v85, v85\n" \
  "v_exp_f32 v86, v86\n" \
  "v_exp_f32 v87, v87\n" \
  "s_waitcnt lgkmcnt(3)\n" \
  "v_mfma_f32_32x32x16_bf16 v[64:79], v[108:111], %6, v[64:79]\n" \
  "ds_read_b32 v108, v121 offset:17024\n" \
  "v_exp_f32 v88, v88\n" \
  "v_cvt_pk_bf16_f32 v96, v80, v81\n" \
  "v_exp_f32 v89, v89\n" \
  "v_cvt_pk_bf16_f32 v97, v82, v83\n" \
  "v_exp_f32 v90, v90\n" \
  "v_cvt_pk_bf16_f32 v98, v84, v85\n" \
  "v_exp_f32 v91, v91\n" \
  "v_cvt_pk_bf16_f32 v99, v86, v87\n" \
  "v_exp_f32 v92, v92\n" \
  "v_exp_f32 v93, v93\n" \
  "s_waitcnt lgkmcnt(3)\n" \
  "v_mfma_f32_32x32x16_bf16 %0, v[112:115], v[96:99], %0\n" \
  "ds_read_b128 v[112:115], v122 offset:14592\n" \
  "v_exp_f32 v94, v94\n" \
  "v_exp_f32 v95, v95\n" \
  "v_cvt_pk_bf16_f32 v100, v88, v89\n" \
  "v_cvt_pk_bf16_f32 v101, v90, v91\n" \
  "v_cvt_pk_bf16_f32 v102, v92, v93\n" \
  "v_cvt_pk_bf16_f32 v103, v94, v95\n" \
  "s_waitcnt lgkmcnt(3)\n" \
  "v_mfma_f32_32x32x16_bf16 v[80:95], v[104:107], %5, %4\n" \
  "ds_read_b128 v[104:107], v120 offset:19456\n" \
  "v_exp_f32 v64, v64\n" \
  "v_exp_f32 v65, v65\n" \
  "v_exp_f32 v66, v66\n" \
  "v_exp_f32 v67, v67\n" \
  "s_waitcnt lgkmcnt(3)\n" \
  "v_mfma_f32_32x32x16_bf16 %0, v[116:119], v[100:103], %0\n" \
  "ds_read_b128 v[116:119], v122 offset:15232\n" \
  "v_exp_f32 v68, v68\n" \
  "v_exp_f32 v69, v69\n" \
  "v_exp_f32 v70, v70\n" \
  "v_exp_f32 v71, v71\n" \
  "s_waitcnt lgkmcnt(3)\n" \
  "v_mfma_f32_32x32x16_bf16 v[80:95], v[108:111], %6, v[80:95]\n" \
  "ds_read_b32 v108, v121 offset:19456\n" \
  "v_exp_f32 v72, v72\n" \
  "v_cvt_pk_bf16_f32 v96, v64, v65\n" \
  "v_exp_f32 v73, v73\n" \
  "v_cvt_pk_bf16_f32 v97, v66, v67\n" \
  "v_exp_f32 v74, v74\n" \
  "v_cvt_pk_bf16_f32 v98, v68, v69\n" \
  "v_exp_f32 v75, v75\n" \
  "v_cvt_pk_bf16_f32 v99, v70, v71\n" \
  "v_exp_f32 v76, v76\n" \
  "v_exp_f32 v77, v77\n" \
  "s_waitcnt lgkmcnt(3)\n" \
  "v_mfma_f32_32x32x16_bf16 %0, v[112:115], v[96:99], %0\n" \
  "ds_read_b128 v[112:115], v122 offset:17024\n" \
  "v_exp_f32 v78, v78\n" \
  "v_exp_f32 v79, v79\n" \
  "v_cvt_pk_bf16_f32 v100, v72, v73\n" \
  "v_cvt_pk_bf16_f32 v101, v74, v75\n" \
  "v_cvt_pk_bf16_f32 v102, v76, v77\n" \
  "v_cvt_pk_bf16_f32 v103, v78, v79\n" \
  "s_waitcnt lgkmcnt(3)\n" \
  "v_mfma_f32_32x32x16_bf16 v[64:79], v[104:107], %5, %4\n" \
  "ds_read_b128 v[104:107], v120 offset:21888\n" \
  "v_exp_f32 v80, v80\n" \
  "v_exp_f32 v81, v81\n" \
  "v_exp_f32 v82, v82\n" \
  "v_exp_f32 v83, v83\n" \
  "s_waitcnt lgkmcnt(3)\n" \
  "v_mfma_f32_32x32x16_bf16 %0, v[116:119], v[100:103], %0\n" \
  "ds_read_b128 v[116:119], v122 offset:17664\n" \
  "v_exp_f32 v84, v84\n" \
  "v_exp_f32 v85, v85\n" \
  "v_exp_f32 v86, v86\n" \
  "v_exp_f32 v87, v87\n" \
  "s_waitcnt lgkmcnt(3)\n" \
  "v_mfma_f32_32x32x16_bf16 v[64:79], v[108:111], %6, v[64:79]\n" \
  "ds_read_b32 v108, v121 offset:21888\n" \
  "v_exp_f32 v88, v88\n" \
  "v_cvt_pk_bf16_f32 v96, v80, v81\n" \
  "v_exp_f32 v89, v89\n" \
  "v_cvt_pk_bf16_f32 v97, v82, v83\n" \
  "v_exp_f32 v90, v90\n" \
  "v_cvt_pk_bf16_f32 v98, v84, v85\n" \
  "v_exp_f32 v91, v91\n" \
  "v_cvt_pk_bf16_f32 v99, v86, v87\n" \
  "v_exp_f32 v92, v92\n" \
  "v_exp_f32 v93, v93\n" \
  "s_waitcnt lgkmcnt(3)\n" \
  "v_mfma_f32_32x32x16_bf16 %0, v[112:115], v[96:99], %0\n" \
  "ds_read_b128 v[112:115], v122 offset:19456\n" \
  "v_exp_f32 v94, v94\n" \
  "v_exp_f32 v95, v95\n" \
  "v_cvt_pk_bf16_f32 v100, v88, v89\n" \
  "v_cvt_pk_bf16_f32 v101, v90, v91\n" \
  "v_cvt_pk_bf16_f32 v102, v92, v93\n" \
  "v_cvt_pk_bf16_f32 v103, v94, v95\n" \
  "s_waitcnt lgkmcnt(3)\n" \
  "v_mfma_f32_32x32x16_bf16 v[80:95], v[104:107], %5, %4\n" \
  "ds_read_b128 v[104:107], v120 offset:24320\n" \
  "v_exp_f32 v64, v64\n" \
  "v_exp_f32 v65, v65\n" \
  "v_exp_f32 v66, v66\n" \
  "v_exp_f32 v67, v67\n" \
  "s_waitcnt lgkmcnt(3)\n" \
  "v_mfma_f32_32x32x16_bf16 %0, v[116:119], v[100:103], %0\n" \
  "ds_read_b128 v[116:119], v122 offset:20096\n" \
  "v_exp_f32 v68, v68\n" \
  "v_exp_f32 v69, v69\n" \
  "v_exp_f32 v70, v70\n" \
  "v_exp_f32 v71, v71\n" \
  "s_waitcnt lgkmcnt(3)\n" \
  "v_mfma_f32_32x32x16_bf16 v[80:95], v[108:111], %6, v[80:95]\n" \
  "ds_read_b32 v108, v121 offset:24320\n" \
  "v_exp_f32 v72, v72\n" \
  "v_cvt_pk_bf16_f32 v96, v64, v65\n" \
  "v_exp_f32 v73, v73\n" \
  "v_cvt_pk_bf16_f32 v97, v66, v67\n" \
  "v_exp_f32 v74, v74\n" \
  "v_cvt_pk_bf16_f32 v98, v68, v69\n" \
  "v_exp_f32 v75, v75\n" \
  "v_cvt_pk_bf16_f32 v99, v70, v71\n" \
  "v_exp_f32 v76, v76\n" \
  "v_exp_f32 v77, v77\n" \
  "s_waitcnt lgkmcnt(3)\n" \
  "v_mfma_f32_32x32x16_bf16 %0, v[112:115], v[96:99], %0\n" \
  "ds_read_b128 v[112:115], v122 offset:21888\n" \
  "v_exp_f32 v78, v78\n" \
  "v_exp_f32 v79, v79\n" \
  "v_cvt_pk_bf16_f32 v100, v72, v73\n" \
  "v_cvt_pk_bf16_f32 v101, v74, v75\n" \
  "v_cvt_pk_bf16_f32 v102, v76, v77\n" \
  "v_cvt_pk_bf16_f32 v103, v78, v79\n" \
  "s_waitcnt lgkmcnt(3)\n" \
  "v_mfma_f32_32x32x16_bf16 v[64:79], v[104:107], %5, %4\n" \
  "ds_read_b128 v[104:107], v120 offset:26752\n" \
  "v_exp_f32 v80, v80\n" \
  "v_exp_f32 v81, v81\n" \
  "v_exp_f32 v82, v82\n" \
  "v_exp_f32 v83, v83\n" \
  "s_waitcnt lgkmcnt(3)\n" \
  "v_mfma_f32_32x32x16_bf16 %0, v[116:119], v[100:103], %0\n" \
  "ds_read_b128 v[116:119], v122 offset:22528\n" \
  "v_exp_f32 v84, v84\n" \
  "v_exp_f32 v85, v85\n" \
  "v_exp_f32 v86, v86\n" \
  "v_exp_f32 v87, v87\n" \
  "s_waitcnt lgkmcnt(3)\n" \
  "v_mfma_f32_32x32x16_bf16 v[64:79], v[108:111], %6, v[64:79]\n" \
  "ds_read_b32 v108, v121 offset:26752\n" \
  "v_exp_f32 v88, v88\n" \
  "v_cvt_pk_bf16_f32 v96, v80, v81\n" \
  "v_exp_f32 v89, v89\n" \
  "v_cvt_pk_bf16_f32 v97, v82, v83\n" \
  "v_exp_f32 v90, v90\n" \
  "v_cvt_pk_bf16_f32 v98, v84, v85\n" \
  "v_exp_f32 v91, v91\n" \
  "v_cvt_pk_bf16_f32 v99, v86, v87\n" \
  "v_exp_f32 v92, v92\n" \
  "v_exp_f32 v93, v93\n" \
  "s_waitcnt lgkmcnt(3)\n" \
  "v_mfma_f32_32x32x16_bf16 %0, v[112:115], v[96:99], %0\n" \
  "ds_read_b128 v[112:115], v122 offset:24320\n" \
  "v_exp_f32 v94, v94\n" \
  "v_exp_f32 v95, v95\n" \
  "v_cvt_pk_bf16_f32 v100, v88, v89\n" \
  "v_cvt_pk_bf16_f32 v101, v90, v91\n" \
  "v_cvt_pk_bf16_f32 v102, v92, v93\n" \
  "v_cvt_pk_bf16_f32 v103, v94, v95\n" \
  "s_waitcnt lgkmcnt(3)\n" \
  "v_mfma_f32_32x32x16_bf16 v[80:95], v[104:107], %5, %4\n" \
  "ds_read_b128 v[104:107], v120 offset:29184\n" \
  "v_exp_f32 v64, v64\n" \
  "v_exp_f32 v65, v65\n" \
  "v_exp_f32 v66, v66\n" \
  "v_exp_f32 v67, v67\n" \
  "s_waitcnt lgkmcnt(3)\n" \
  "v_mfma_f32_32x32x16_bf16 %0, v[116:119], v[100:103], %0\n" \
  "ds_read_b128 v[116:119], v122 offset:24960\n" \
  "v_exp_f32 v68, v68\n" \
  "v_exp_f32 v69, v69\n" \
  "v_exp_f32 v70, v70\n" \
  "v_exp_f32 v71, v71\n" \
  "s_waitcnt lgkmcnt(3)\n" \
  "v_mfma_f32_32x32x16_bf16 v[80:95], v[108:111], %6, v[80:95]\n" \
  "ds_read_b32 v108, v121 offset:29184\n" \
  "v_exp_f32 v72, v72\n" \
  "v_cvt_pk_bf16_f32 v96, v64, v65\n" \
  "v_exp_f32 v73, v73\n" \
  "v_cvt_pk_bf16_f32 v97, v66, v67\n" \
  "v_exp_f32 v74, v74\n" \
  "v_cvt_pk_bf16_f32 v98, v68, v69\n" \
  "v_exp_f32 v75, v75\n" \
  "v_cvt_pk_bf16_f32 v99, v70, v71\n" \
  "v_exp_f32 v76, v76\n" \
  "v_exp_f32 v77, v77\n" \
  "s_waitcnt lgkmcnt(3)\n" \
  "v_mfma_f32_32x32x16_bf16 %0, v[112:115], v[96:99], %0\n" \
  "ds_read_b128 v[112:115], v122 offset:26752\n" \
  "v_exp_f32 v78, v78\n" \
  "v_exp_f32 v79, v79\n" \
  "v_cvt_pk_bf16_f32 v100, v72, v73\n" \
  "v_cvt_pk_bf16_f32 v101, v74, v75\n" \
  "v_cvt_pk_bf16_f32 v102, v76, v77\n" \
  "v_cvt_pk_bf16_f32 v103, v78, v79\n" \
  "s_waitcnt lgkmcnt(3)\n" \
  "v_mfma_f32_32x32x16_bf16 v[64:79], v[104:107], %5, %4\n" \
  "ds_read_b128 v[104:107], v120 offset:31616\n" \
  "v_exp_f32 v80, v80\n" \
  "v_exp_f32 v81, v81\n" \
  "v_exp_f32 v82, v82\n" \
  "v_exp_f32 v83, v83\n" \
  "s_waitcnt lgkmcnt(3)\n" \
  "v_mfma_f32_32x32x16_bf16 %0, v[116:119], v[100:103], %0\n" \
  "ds_read_b128 v[116:119], v122 offset:27392\n" \
  "v_exp_f32 v84, v84\n" \
  "v_exp_f32 v85, v85\n" \
  "v_exp_f32 v86, v86\n" \
  "v_exp_f32 v87, v87\n" \
  "s_waitcnt lgkmcnt(3)\n" \
  "v_mfma_f32_32x32x16_bf16 v[64:79], v[108:111], %6, v[64:79]\n" \
  "ds_read_b32 v108, v121 offset:31616\n" \
  "v_exp_f32 v88, v88\n" \
  "v_cvt_pk_bf16_f32 v96, v80, v81\n" \
  "v_exp_f32 v89, v89\n" \
  "v_cvt_pk_bf16_f32 v97, v82, v83\n" \
  "v_exp_f32 v90, v90\n" \
  "v_cvt_pk_bf16_f32 v98, v84, v85\n" \
  "v_exp_f32 v91, v91\n" \
  "v_cvt_pk_bf16_f32 v99, v86, v87\n" \
  "v_exp_f32 v92, v92\n" \
  "v_exp_f32 v93, v93\n" \
  "s_waitcnt lgkmcnt(3)\n" \
  "v_mfma_f32_32x32x16_bf16 %0, v[112:115], v[96:99], %0\n" \
  "ds_read_b128 v[112:115], v122 offset:29184\n" \
  "v_exp_f32 v94, v94\n" \
  "v_exp_f32 v95, v95\n" \
  "v_cvt_pk_bf16_f32 v100, v88, v89\n" \
  "v_cvt_pk_bf16_f32 v101, v90, v91\n" \
  "v_cvt_pk_bf16_f32 v102, v92, v93\n" \
  "v_cvt_pk_bf16_f32 v103, v94, v95\n" \
  "s_waitcnt lgkmcnt(3)\n" \
  "v_mfma_f32_32x32x16_bf16 v[80:95], v[104:107], %5, %4\n" \
  "ds_read_b128 v[104:107], v120 offset:34048\n" \
  "v_exp_f32 v64, v64\n" \
  "v_exp_f32 v65, v65\n" \
  "v_exp_f32 v66, v66\n" \
  "v_exp_f32 v67, v67\n" \
  "s_waitcnt lgkmcnt(3)\n" \
  "v_mfma_f32_32x32x16_bf16 %0, v[116:119], v[100:103], %0\n" \
  "ds_read_b128 v[116:119], v122 offset:29824\n" \
  "v_exp_f32 v68, v68\n" \
  "v_exp_f32 v69, v69\n" \
  "v_exp_f32 v70, v70\n" \
  "v_exp_f32 v71, v71\n" \
  "s_waitcnt lgkmcnt(3)\n" \
  "v_mfma_f32_32x32x16_bf16 v[80:95], v[108:111], %6, v[80:95]\n" \
  "ds_read_b32 v108, v121 offset:34048\n" \
  "v_exp_f32 v72, v72\n" \
  "v_cvt_pk_bf16_f32 v96, v64, v65\n" \
  "v_exp_f32 v73, v73\n" \
  "v_cvt_pk_bf16_f32 v97, v66, v67\n" \
  "v_exp_f32 v74, v74\n" \
  "v_cvt_pk_bf16_f32 v98, v68, v69\n" \
  "v_exp_f32 v75, v75\n" \
  "v_cvt_pk_bf16_f32 v99, v70, v71\n" \
  "v_exp_f32 v76, v76\n" \
  "v_exp_f32 v77, v77\n" \
  "s_waitcnt lgkmcnt(3)\n" \
  "v_mfma_f32_32x32x16_bf16 %0, v[112:115], v[96:99], %0\n" \
  "ds_read_b128 v[112:115], v122 offset:31616\n" \
  "v_exp_f32 v78, v78\n" \
  "v_exp_f32 v79, v79\n" \
  "v_cvt_pk_bf16_f32 v100, v72, v73\n" \
  "v_cvt_pk_bf16_f32 v101, v74, v75\n" \
  "v_cvt_pk_bf16_f32 v102, v76, v77\n" \
  "v_cvt_pk_bf16_f32 v103, v78, v79\n" \
  "s_waitcnt vmcnt(0)\n" \
  "s_barrier\n" \
  "s_waitcnt lgkmcnt(3)\n" \
  "v_mfma_f32_32x32x16_bf16 v[64:79], v[104:107], %5, %4\n" \
  "ds_read_b128 v[104:107], v120 offset:36480\n" \
  "v_exp_f32 v80, v80\n" \
  "v_exp_f32 v81, v81\n" \
  "v_exp_f32 v82, v82\n" \
  "v_exp_f32 v83, v83\n" \
  "s_waitcnt lgkmcnt(3)\n" \
  "v_mfma_f32_32x32x16_bf16 %0, v[116:119], v[100:103], %0\n" \
  "ds_read_b128 v[116:119], v122 offset:32256\n" \
  "v_exp_f32 v84, v84\n" \
  "v_exp_f32 v85, v85\n" \
  "v_exp_f32 v86, v86\n" \
  "v_exp_f32 v87, v87\n" \
  "s_waitcnt lgkmcnt(3)\n" \
  "v_mfma_f32_32x32x16_bf16 v[64:79], v[108:111], %6, v[64:79]\n" \
  "ds_read_b32 v108, v121 offset:36480\n" \
  "v_exp_f32 v88, v88\n" \
  "v_cvt_pk_bf16_f32 v96, v80, v81\n" \
  "v_exp_f32 v89, v89\n" \
  "v_cvt_pk_bf16_f32 v97, v82, v83\n" \
  "v_exp_f32 v90, v90\n" \
  "v_cvt_pk_bf16_f32 v98, v84, v85\n" \
  "v_exp_f32 v91, v91\n" \
  "v_cvt_pk_bf16_f32 v99, v86, v87\n" \
  "v_exp_f32 v92, v92\n" \
  "v_exp_f32 v93, v93\n" \
  "s_waitcnt lgkmcnt(3)\n" \
  "v_mfma_f32_32x32x16_bf16 %0, v[112:115], v[96:99], %0\n" \
  "ds_read_b128 v[112:115], v122 offset:34048\n" \
  "v_exp_f32 v94, v94\n" \
  "v_exp_f32 v95, v95\n" \
  "v_cvt_pk_bf16_f32 v100, v88, v89\n" \
  "v_cvt_pk_bf16_f32 v101, v90, v91\n" \
  "v_cvt_pk_bf16_f32 v102, v92, v93\n" \
  "v_cvt_pk_bf16_f32 v103, v94, v95\n" \
  "s_waitcnt lgkmcnt(3)\n" \
  "v_mfma_f32_32x32x16_bf16 v[80:95], v[104:107], %5, %4\n" \
  "ds_read_b128 v[104:107], v123 offset:0\n" \
  "v_exp_f32 v64, v64\n" \
  "v_exp_f32 v65, v65\n" \
  "v_exp_f32 v66, v66\n" \
  "v_exp_f32 v67, v67\n" \
  "s_waitcnt lgkmcnt(3)\n" \
  "v_mfma_f32_32x32x16_bf16 %0, v[116:119], v[100:103], %0\n" \
  "ds_read_b128 v[116:119], v122 offset:34688\n" \
  "v_exp_f32 v68, v68\n" \
  "v_exp_f32 v69, v69\n" \
  "v_exp_f32 v70, v70\n" \
  "v_exp_f32 v71, v71\n" \
  "s_waitcnt lgkmcnt(3)\n" \
  "v_mfma_f32_32x32x16_bf16 v[80:95], v[108:111], %6, v[80:95]\n" \
  "ds_read_b32 v108, v124 offset:0\n" \
  "v_exp_f32 v72, v72\n" \
  "v_cvt_pk_bf16_f32 v96, v64, v65\n" \
  "v_exp_f32 v73, v73\n" \
  "v_cvt_pk_bf16_f32 v97, v66, v67\n" \
  "v_exp_f32 v74, v74\n" \
  "v_cvt_pk_bf16_f32 v98, v68, v69\n" \
  "v_exp_f32 v75, v75\n" \
  "v_cvt_pk_bf16_f32 v99, v70, v71\n" \
  "v_exp_f32 v76, v76\n" \
  "v_exp_f32 v77, v77\n" \
  "s_waitcnt lgkmcnt(3)\n" \
  "v_mfma_f32_32x32x16_bf16 %0, v[112:115], v[96:99], %0\n" \
  "ds_read_b128 v[112:115], v122 offset:36480\n" \
  "v_exp_f32 v78, v78\n" \
  "v_exp_f32 v79, v79\n" \
  "v_cvt_pk_bf16_f32 v100, v72, v73\n" \
  "v_cvt_pk_bf16_f32 v101, v74, v75\n" \
  "v_cvt_pk_bf16_f32 v102, v76, v77\n" \
  "v_cvt_pk_bf16_f32 v103, v78, v79\n" \
  "s_waitcnt lgkmcnt(3)\n" \
  "v_mfma_f32_32x32x16_bf16 v[64:79], v[104:107], %5, %4\n" \
  "ds_read_b128 v[104:107], v123 offset:2432\n" \
  "v_exp_f32 v80, v80\n" \
  "v_exp_f32 v81, v81\n" \
  "v_exp_f32 v82, v82\n" \
  "v_exp_f32 v83, v83\n" \
  "s_waitcnt lgkmcnt(3)\n" \
  "v_mfma_f32_32x32x16_bf16 %0, v[116:119], v[100:103], %0\n" \
  "ds_read_b128 v[116:119], v122 offset:37120\n" \
  "v_exp_f32 v84, v84\n" \
  "v_exp_f32 v85, v85\n" \
  "v_exp_f32 v86, v86\n" \
  "v_exp_f32 v87, v87\n" \
  "s_waitcnt lgkmcnt(3)\n" \
  "v_mfma_f32_32x32x16_bf16 v[64:79], v[108:111], %6, v[64:79]\n" \
  "ds_read_b32 v108, v124 offset:2432\n" \
  "v_exp_f32 v88, v88\n" \
  "v_cvt_pk_bf16_f32 v96, v80, v81\n" \
  "v_exp_f32 v89, v89\n" \
  "v_cvt_pk_bf16_f32 v97, v82, v83\n" \
  "v_exp_f32 v90, v90\n" \
  "v_cvt_pk_bf16_f32 v98, v84, v85\n" \
  "v_exp_f32 v91, v91\n" \
  "v_cvt_pk_bf16_f32 v99, v86, v87\n" \
  "v_exp_f32 v92, v92\n" \
  "v_exp_f32 v93, v93\n" \
  "s_waitcnt lgkmcnt(3)\n" \
  "v_mfma_f32_32x32x16_bf16 %0, v[112:115], v[96:99], %0\n" \
  "ds_read_b128 v[112:115], v125 offset:0\n" \
  "v_exp_f32 v94, v94\n" \
  "v_exp_f32 v95, v95\n" \
  "v_cvt_pk_bf16_f32 v100, v88, v89\n" \
  "v_cvt_pk_bf16_f32 v101, v90, v91\n" \
  "v_cvt_pk_bf16_f32 v102, v92, v93\n" \
  "v_cvt_pk_bf16_f32 v103, v94, v95\n" \
  "v_swap_b32 v120, v123\n" \
  "v_swap_b32 v121, v124\n" \
  "v_swap_b32 v122, v125\n" \
  "s_xor_b32 %3, %3, 38912\n" \
  "s_add_u32 %2, %2, 1\n" \
  "s_cmp_lg_u32 %2, 4\n" \
  "s_cbranch_scc1 1b\n" \
  "s_waitcnt lgkmcnt(0)\n" \
  "s_nop 1\n" \
  "v_mfma_f32_32x32x16_bf16 %0, v[116:119], v[100:103], %0\n" \
  "s_mov_b32 m0, %1\n" \
  "s_nop 7\n" \
  "s_nop 3\n" \
  ""
#define ATTN_LOOP_CLOBBERS "v61", "v62", "v63", "v64", "v65", "v66", "v67", "v68", "v69", "v70", "v71", "v72", "v73", "v74", "v75", "v76", "v77", "v78", "v79", "v80", "v81", "v82", "v83", "v84", "v85", "v86", "v87", "v88", "v89", "v90", "v91", "v92", "v93", "v94", "v95", "v96", "v97", "v98", "v99", "v100", "v101", "v102", "v103", "v104", "v105", "v106", "v107", "v108", "v109", "v110", "v111", "v112", "v113", "v114", "v115", "v116", "v117", "v118", "v119", "v120", "v121", "v122", "v123", "v124", "v125", "scc"

DI bf16x8 pack8(const f32x16& S, int o) {
  u32x4 p;
#pragma unroll
  for (int e = 0; e < 4; ++e)
    p[e] = cvtpk(__builtin_amdgcn_exp2f(S[o + 2 * e]), __builtin_amdgcn_exp2f(S[o + 2 * e + 1]));
  return __builtin_bit_cast(bf16x8, p);
}

DI f32x16 attn_safe_pass(const char* kv, bf16x8 q0, bf16x8 q1, int koff, int toff, int voff) {
  f32x16 O, z;
#pragma unroll
  for (int i = 0; i < 16; ++i) { O[i] = 0.f; z[i] = 0.f; }
  float m = -INFINITY;
  for (int tile = 0; tile < 64; ++tile) {
    const char* tb = kv + tile * KV_TILE;
    const bf16x8 kf = *(const bf16x8*)(tb + koff);
    const u32x4 kt4 = u32x4{*(const unsigned*)(tb + toff), 0u, 0u, 0u};
    const bf16x8 vf0 = *(const bf16x8*)(tb + voff);
    const bf16x8 vf1 = *(const bf16x8*)(tb + voff + 640);
    f32x16 S = MFMA32(kf, q0, z);
    S = MFMA32(__builtin_bit_cast(bf16x8, kt4), q1, S);
    float tm = max16(S);
    tm = fmaxf(tm, __shfl_xor(tm, 32));
    const float mn = fmaxf(m, tm);
    const float alpha = __builtin_amdgcn_exp2f(m - mn);
    m = mn;
#pragma unroll
    for (int i = 0; i < 16; ++i) { O[i] *= alpha; S[i] -= mn; }
    O = MFMA32(vf0, pack8(S, 0), O);
    O = MFMA32(vf1, pack8(S, 8), O);
  }
  return O;
}

__global__ void __launch_bounds__(512, 4) attn_kernel(const u16* __restrict__ wo, char* __restrict__ ws) {
  extern __shared__ __attribute__((aligned(16))) char smem[];
  if (blockIdx.x >= 256) {
    conv_body<false>(blockIdx.x - 256, smem, ws);
    return;
  }
  const int tid = threadIdx.x, lane = tid & 63, w = tid >> 6;
  const int c = lane & 31, h = lane >> 5;
  const char* kvsrc = ws + WS_KV;
  const int token = ((blockIdx.x & 7) * 32 + (blockIdx.x >> 3)) * 8 + w;
  u32x4 qa;
  unsigned qb, pfv;
  {
    const unsigned* qp = (const unsigned*)((const u16*)(ws + WS_Q) + (size_t)token * DM + c * DH);
    asm volatile("global_load_dwordx4 %0, %2, off\n\tglobal_load_dword %1, %3, off offset:32"
                 : "=&v"(qa), "=&v"(qb) : "v"(qp + 4 * h), "v"(qp) : "memory");
  }
  {
    const int ln = (blockIdx.x >> 3) * 162 + (tid < 162 ? tid : 0);
    pfv = *(const unsigned*)((const char*)wo + (size_t)ln * 128);
  }
  const unsigned lbase = (unsigned)(size_t)smem;
  const int wu = __builtin_amdgcn_readfirstlane(w);
  const int p4 = wu + 32 < 38 ? wu + 32 : 37;
#pragma unroll
  for (int ch = 0; ch < 2; ++ch) {
#pragma unroll
    for (int i = 0; i < 5; ++i) {
      const int pc = i < 4 ? wu + 8 * i : p4;
      dma16(kvsrc + ch * AT_CHUNK + pc * 1024 + lane * 16, lbase + ch * AT_CHUNK + pc * 1024);
    }
  }
  asm volatile("s_waitcnt vmcnt(8)\n\ts_barrier" : "+v"(qa), "+v"(qb)::"memory");
  const bf16x8 q0 = __builtin_bit_cast(bf16x8, qa);
  const bf16x8 q1 = __builtin_bit_cast(bf16x8, u32x4{h == 0 ? qb : 0u, 0u, 0u, 0u});

  const int dv = c < 19 ? c : 19;
  const int koff = KV_KP + h * 512 + c * 16;
  const int toff = KV_KT + c * 4;
  const int voff = KV_V + h * 320 + dv * 16;

  f32x16 negm, O;
  u32x4 ktA = u32x4{0u, 0u, 0u, 0u};
  {
    const bf16x8 kf = *(const bf16x8*)(smem + koff);
    ktA[0] = *(const unsigned*)(smem + toff);
    f32x16 z;
#pragma unroll
    for (int i = 0; i < 16; ++i) z[i] = 0.f;
    f32x16 S = MFMA32(kf, q0, z);
    S = MFMA32(__builtin_bit_cast(bf16x8, ktA), q1, S);
    float mx = max16(S);
    mx = fmaxf(mx, __shfl_xor(mx, 32));
#pragma unroll
    for (int i = 0; i < 16; ++i) { negm[i] = -mx; O[i] = 0.f; }
  }
  {
    unsigned s_m0, s_cnt, s_oth;
    asm volatile(ATTN_LOOP_ASM
                 : "+v"(O), "=&s"(s_m0), "=&s"(s_cnt), "=&s"(s_oth)
                 : "v"(negm), "v"(q0), "v"(q1), "v"(lbase + koff), "v"(lbase + toff), "v"(lbase + voff), "s"(kvsrc),
                   "v"(2 * AT_CHUNK + wu * 1024 + lane * 16), "v"(2 * AT_CHUNK + p4 * 1024 + lane * 16),
                   "s"(lbase + wu * 1024), "s"(lbase + p4 * 1024)
                 : "memory", ATTN_LOOP_CLOBBERS);
  }
  asm volatile("" ::"v"(pfv));
  {
    bool ok = true;
#pragma unroll
    for (int i = 0; i < 11; ++i) ok = ok && (fabsf(O[i]) < 1.0e30f);
    ok = ok && (h != 0 || O[10] < 1.8e19f);
    if (!__all(ok)) O = attn_safe_pass(kvsrc, q0, q1, koff, toff, voff);
  }
  {
    const float l = __shfl(O[10], c);
    const float inv = 1.f / l;
    unsigned* op = (unsigned*)((u16*)(ws + WS_O) + (size_t)token * DM + c * DH);
    op[2 * h + 0] = pack2(O[0] * inv, O[1] * inv);
    op[2 * h + 1] = pack2(O[2] * inv, O[3] * inv);
    op[4 + 2 * h + 0] = pack2(O[4] * inv, O[5] * inv);
    op[4 + 2 * h + 1] = pack2(O[6] * inv, O[7] * inv);
    if (h == 0) op[8] = pack2(O[8] * inv, O[9] * inv);
  }
}

constexpr int PC_LDS = GM_LDS;
constexpr int PC_CONV = DM - CV_SPLIT;
static_assert(CV_LDS <= PC_LDS && PC_CONV % 8 == 0 && CV_SPLIT % 64 == 0, "kernel C layout");
__global__ void __launch_bounds__(512, 4) out_kernel(const u16* __restrict__ x, const u16* __restrict__ wo,
                                                     const u16* __restrict__ dskip, char* __restrict__ ws,
                                                     u16* __restrict__ out) {
  extern __shared__ __attribute__((aligned(16))) char smem[];
  if (blockIdx.x < PC_CONV) {
    conv_body<true>(CV_SPLIT + blockIdx.x, smem, ws);
    return;
  }
  const int tid = threadIdx.x, lane = tid & 63, w = tid >> 6;
  const int j8 = (blockIdx.x - PC_CONV) >> 3;
  const int rb = (blockIdx.x & 7) * 2 + (j8 & 1), cb = j8 >> 1;
  const int m = lane & 31, h = lane >> 5;
  const int token = rb * 128 + 32 * (w & 3) + m;
  const int col0 = cb * 64 + 32 * (w >> 2) + 4 * h;
  const float gg = ((const float*)(ws + WS_G))[token];
  s16x4 uv[4], dv[4];
  u16 yv[16];
#pragma unroll
  for (int g = 0; g < 4; ++g) {
    uv[g] = *(const s16x4*)(x + (size_t)token * DM + col0 + 8 * g);
    dv[g] = *(const s16x4*)(dskip + col0 + 8 * g);
  }
  const bool late = cb * 64 >= CV_SPLIT;
  const u16* yp = (const u16*)(ws + (late ? WS_Y2 : WS_Y)) + (size_t)(cb * 64 + (tid >> 3)) * L_SEQ + rb * 128 + (tid & 7) * 8;
  u32x4 y0 = u32x4{0u, 0u, 0u, 0u}, y1 = u32x4{0u, 0u, 0u, 0u};
  if (!late) {
    y0 = *(const u32x4*)yp;
    y1 = *(const u32x4*)(yp + 64);
  }
  const f32x16 acc = gemm_tile<false>((const u16*)(ws + WS_O) + (size_t)rb * 128 * DM, wo + cb * 64, nullptr, nullptr, smem);
  if (late) {
    if (tid == 0) {
      const unsigned* cnt = (const unsigned*)(ws + WS_CNT) + cb * 32;
      while (__hip_atomic_load(cnt, __ATOMIC_RELAXED, __HIP_MEMORY_SCOPE_AGENT) < 64u) __builtin_amdgcn_s_sleep(2);
    }
    __syncthreads();
    asm volatile("global_load_dwordx4 %0, %2, off sc0 sc1\n\tglobal_load_dwordx4 %1, %2, off offset:128 sc0 sc1\n\ts_waitcnt vmcnt(0)"
                 : "=&v"(y0), "=&v"(y1) : "v"(yp) : "memory");
  }
  __syncthreads();
  {
    u16* yl = (u16*)smem;
    *(u32x4*)(yl + (tid >> 3) * 136 + (tid & 7) * 8) = y0;
    *(u32x4*)(yl + (tid >> 3) * 136 + (tid & 7) * 8 + 64) = y1;
    __syncthreads();
    const u16* yr = yl + (32 * (w >> 2) + 4 * h) * 136 + 32 * (w & 3) + m;
#pragma unroll
    for (int i = 0; i < 16; ++i) yv[i] = yr[(8 * (i >> 2) + (i & 3)) * 136];
  }
  const float og = bfr(1.f - gg);
#pragma unroll
  for (int g = 0; g < 4; ++g) {
    float r[4];
#pragma unroll
    for (int i = 0; i < 4; ++i) {
      const float y = bf2f(yv[4 * g + i]);
      const float a = bfr(y + bfr(bf2f((u16)uv[g][i]) * bf2f((u16)dv[g][i])));
      const float b = bfr(acc[4 * g + i]);
      r[i] = bfr(mul1(gg, a)) + bfr(mul1(og, b));
    }
    u32x2 v;
    v[0] = pack2(r[0], r[1]);
    v[1] = pack2(r[2], r[3]);
    *(u32x2*)(out + (size_t)token * DM + col0 + 8 * g) = v;
  }
}

extern "C" void kernel_launch(void* const* d_in, const int* in_sizes, int n_in, void* d_out, int out_size,
                              void* d_ws, size_t ws_size, hipStream_t stream) {
  const u16* x = (const u16*)d_in[0];
  const u16* logA = (const u16*)d_in[1];
  const u16* Aim = (const u16*)d_in[2];
  const u16* Cre = (const u16*)d_in[3];
  const u16* Cim = (const u16*)d_in[4];
  const u16* logdt = (const u16*)d_in[5];
  const u16* dskip = (const u16*)d_in[6];
  const u16* wq = (const u16*)d_in[8];
  const u16* wk = (const u16*)d_in[9];
  const u16* wv = (const u16*)d_in[10];
  const u16* wo = (const u16*)d_in[11];
  const u16* gw = (const u16*)d_in[12];
  char* ws = (char*)d_ws;
  (void)hipFuncSetAttribute((const void*)proj_kernel, hipFuncAttributeMaxDynamicSharedMemorySize, PA_LDS);
  (void)hipFuncSetAttribute((const void*)attn_kernel, hipFuncAttributeMaxDynamicSharedMemorySize, AT_LDS);
  (void)hipFuncSetAttribute((const void*)out_kernel, hipFuncAttributeMaxDynamicSharedMemorySize, PC_LDS);
  proj_kernel<<<224 + DM / 2, 512, PA_LDS, stream>>>(x, wq, wk, wv, gw, logA, Aim, Cre, Cim, logdt, ws);
  attn_kernel<<<256 + CV_SPLIT, 512, AT_LDS, stream>>>(wo, ws);
  out_kernel<<<PC_CONV + 144, 512, PC_LDS, stream>>>(x, wo, dskip, ws, (u16*)d_out);
}
```
